```python
import torch

def _softmax(x, axis=-1):
    return torch.softmax(x, dim=axis)

def _gelu(x):
    return torch.nn.functional.gelu(x, approximate='tanh')

def _silu(x):
    return torch.nn.functional.silu(x)

def _sigmoid(x):
    return torch.sigmoid(x)

def _layernorm(x, axis=-1, eps=1e-05):
    m = x.mean(dim=axis, keepdim=True)
    v = x.var(dim=axis, keepdim=True, unbiased=False)
    return (x - m) / torch.sqrt(v + eps)

def _scatter_add(tgt, idx, src, axis=0):
    return tgt.clone().index_add_(axis, idx, src)

def _segment_sum(src, seg, n):
    out = torch.zeros((n,) + src.shape[1:], dtype=src.dtype)
    return out.index_add_(0, seg, src)

def _pad(x, pw):
    flat = tuple((v for pair in reversed(pw) for v in pair))
    return torch.nn.functional.pad(x, flat)
kernel_name = 'adaLN_cond__sliding_local_dilated__dense_activation_relu__prompt'

def setup_inputs():

    _w = lambda a: (lambda t: t.to(torch.bfloat16) if t.is_floating_point() else t)(torch.as_tensor(a))

    def _A_setup():
        B, S, D = (1, 2048, 512)
        return {'x': _w((lambda *s: torch.randn(*s, dtype=torch.bfloat16) * 0.05)(B, S, D)), 'wqkv': _w((lambda *s: torch.randn(*s, dtype=torch.bfloat16) * 0.05)(D, 3 * D)), 'wo': _w((lambda *s: torch.randn(*s, dtype=torch.bfloat16) * 0.05)(D, D))}
    _A_in = _A_setup()

    def _B_setup():
        B, S, D, Dff = (1, 2048, 512, 1376)
        return {'x': _w((lambda *s: torch.randn(*s, dtype=torch.bfloat16) * 0.05)(B, S, D)), 'w1': _w((lambda *s: torch.randn(*s, dtype=torch.bfloat16) * 0.05)(D, Dff)), 'w2': _w((lambda *s: torch.randn(*s, dtype=torch.bfloat16) * 0.05)(Dff, D))}
    _B_in = _B_setup()
    return {'A_x': _A_in['x'], 'A_wqkv': _A_in['wqkv'], 'A_wo': _A_in['wo'], 'B_x': _B_in['x'], 'B_w1': _B_in['w1'], 'B_w2': _B_in['w2'], 'c': _w((lambda *s: torch.randn(*s, dtype=torch.bfloat16) * 0.1)(1, 512) * 8.0), 'w_mod': _w((lambda *s: torch.randn(*s, dtype=torch.bfloat16) * 0.1)(512, 6 * 512) * 512 ** (-0.5) * 8.0)}

def reference(A_x, A_wqkv, A_wo, B_x, B_w1, B_w2, c, w_mod):

    def _A_ref(x, wqkv, wo):
        B, S, D = x.shape
        H = 32
        Dh = D // H
        W, dil = (8, 2)
        qkv = (lambda x, *d: x.permute(*d))((x @ wqkv).reshape(B, S, 3, H, Dh), 2, 0, 3, 1, 4)
        q, k, v = (qkv[0], qkv[1], qkv[2])
        scale = Dh ** (-0.5)
        pos = torch.arange(S)
        off = torch.arange(W) * dil
        idx = pos[:, None] - off[None, :]
        mask = idx >= 0
        idx_c = torch.where(mask, idx, 0).reshape(-1)
        kg = k[:, :, idx_c, :].reshape(B, H, S, W, Dh)
        vg = v[:, :, idx_c, :].reshape(B, H, S, W, Dh)
        logits = torch.einsum('bhsd,bhswd->bhsw', q, kg) * scale
        logits = torch.where(mask[None, None, :, :], logits, -1000000000.0)
        attn = _softmax(logits, axis=-1)
        o = torch.einsum('bhsw,bhswd->bhsd', attn, vg)
        return (lambda x, *d: x.permute(*d))(o, 0, 2, 1, 3).reshape(B, S, D) @ wo

    def _B_ref(x, w1, w2):
        h = torch.einsum('bsd,df->bsf', x, w1)
        zero = torch.zeros_like(h)
        h = torch.maximum(h, zero)
        return torch.einsum('bsf,fd->bsd', h, w2)
    x = A_x
    mod = (_silu(c) @ w_mod).reshape(c.shape[0], 6, 1, -1)
    sa, ca, ga, sb, cb, gb = (mod[:, 0].clone(), mod[:, 1].clone(), mod[:, 2].clone(), mod[:, 3].clone(), mod[:, 4].clone(), mod[:, 5].clone())
    h = _layernorm(x, axis=-1) * (1 + ca) + sa
    x = x + ga * _A_ref(h, A_wqkv, A_wo)
    h = _layernorm(x, axis=-1) * (1 + cb) + sb
    x = x + gb * _B_ref(h, B_w1, B_w2)
    return x
```

```cpp
#include <hip/hip_runtime.h>
#include <stdint.h>

typedef unsigned short u16;

__global__ void __launch_bounds__(256, 1)
fused_block(const u16* x, const u16* wqkv, const u16* wo, const u16* w1, const u16* w2, const u16* cvec, const u16* wmod, u16* out,
            unsigned char* ws) {
    __shared__ __attribute__((aligned(16))) unsigned char smem[163840];
    unsigned tid = threadIdx.x;
    asm volatile("" :: "v"((__attribute__((address_space(3))) unsigned char*)smem));
    asm volatile(
        "s_nop 1\n\t"
        "s_load_dwordx16 s[4:19], %1, 0\n\t"
        "s_load_dwordx2 s[20:21], %1, 64\n\t"
        "v_lshrrev_b32 v6, 6, %0\n\t"
        "v_and_b32 v1, 63, %0\n\t"
        "v_readfirstlane_b32 s24, v6\n\t"
        "s_and_b32 s25, %2, 7\n\t"
        "s_lshr_b32 s26, %2, 3\n\t"
        "v_lshlrev_b32 v2, 4, v1\n\t"
        "s_and_b32 s27, s26, 7\n\t"
        "s_lshr_b32 s28, s26, 3\n\t"
        "s_lshl_b32 s29, s25, 2\n\t"
        "s_add_u32 s29, s29, s28\n\t"
        "s_lshl_b32 s30, s29, 6\n\t"
        "s_getpc_b64 s[40:41]\n\t"
        ".Lpf_here:\n\t"
        "s_lshl_b32 s31, s26, 2\n\t"
        "s_add_u32 s31, s31, s24\n\t"
        "s_lshl_b32 s31, s31, 10\n\t"
        "v_add_u32 v6, s31, v2\n\t"
        "s_mov_b32 s33, fused_code_end-.Lpf_here-16\n\t"
        "s_and_b32 s40, s40, -16\n\t"
        "v_cmp_ge_u32 vcc, s33, v6\n\t"
        "v_lshrrev_b32 v3, 4, v1\n\t"
        "v_and_b32 v4, 15, v1\n\t"
        "s_mov_b64 exec, vcc\n\t"
        "global_load_dwordx4 a[252:255], v6, s[40:41]\n\t"
        "s_mov_b64 exec, -1\n\t"
        "s_mov_b32 s42, 0xffff0000\n\t"
        "s_mov_b32 s43, 0x3f803f80\n\t"
        "s_mul_i32 s31, s25, 24\n\t"
        "s_add_u32 s31, s31, s26\n\t"
        "s_mul_i32 s65, s31, 1366\n\t"
        "s_lshr_b32 s65, s65, 16\n\t"
        "s_mul_i32 s33, s65, 48\n\t"
        "s_sub_u32 s64, s31, s33\n\t"
        "s_cmp_lt_u32 s26, 24\n\t"
        "s_mov_b32 s66, 0x7f000000\n\t"
        "s_cselect_b32 s66, 0, s66\n\t"
        "s_and_b32 s65, s65, 3\n\t"
        "v_lshrrev_b32 v11, 3, v1\n\t"
        "v_and_b32 v7, 7, v1\n\t"
        "v_and_b32 v8, 1, v3\n\t"
        "v_lshlrev_b32 v8, 1, v8\n\t"
        "v_xor_b32 v7, v7, v8\n\t"
        "v_lshlrev_b32 v5, 4, v7\n\t"
        "v_xor_b32 v10, 64, v5\n\t"
        "v_mul_u32_u24 v6, 6144, v11\n\t"
        "v_add_u32 v12, v6, v5\n\t"
        "v_add_u32 v13, v6, v10\n\t"
        "v_and_b32 v6, 127, %0\n\t"
        "s_lshl_b32 s31, s65, 7\n\t"
        "v_add_u32 v7, s31, v6\n\t"
        "v_lshlrev_b32 v7, 1, v7\n\t"
        "v_lshlrev_b32 v15, 1, v6\n\t"
        "v_add_u32 v15, 131072, v15\n\t"
        "s_waitcnt lgkmcnt(0)\n\t"
        "s_mov_b32 s52, s16\n\t"
        "s_and_b32 s53, s17, 0xffff\n\t"
        "s_mov_b32 s54, 0x300000\n\t"
        "s_mov_b32 s55, 0x20000\n\t"
        "global_load_ushort v14, v7, s[14:15]\n\t"
        "s_lshl_b32 s31, s65, 2\n\t"
        "s_add_u32 s31, s31, s24\n\t"
        "s_mul_i32 s31, s31, 196608\n\t"
        "s_lshl_b32 s33, s64, 7\n\t"
        "s_add_u32 s31, s31, s33\n\t"
        "s_add_u32 s31, s31, s66\n\t"
        "s_lshl_b32 s33, s24, 12\n\t"
        "s_add_u32 m0, s33, 147456\n\t"
        "s_nop 0\n\t"
        "buffer_load_dwordx4 v12, s[52:55], s31 offen lds\n\t"
        "s_add_u32 m0, m0, 1024\n\t"
        "s_add_u32 s31, s31, 49152\n\t"
        "buffer_load_dwordx4 v13, s[52:55], s31 offen lds\n\t"
        "s_add_u32 m0, m0, 1024\n\t"
        "s_add_u32 s31, s31, 49152\n\t"
        "buffer_load_dwordx4 v12, s[52:55], s31 offen lds\n\t"
        "s_add_u32 m0, m0, 1024\n\t"
        "s_add_u32 s31, s31, 49152\n\t"
        "buffer_load_dwordx4 v13, s[52:55], s31 offen lds\n\t"
        "v_mov_b32 v8, 0\n\t"
        "global_load_dwordx2 v[16:17], v8, s[20:21] sc1\n\t"
        "s_mov_b32 s44, s4\n\t"
        "s_and_b32 s45, s5, 0xffff\n\t"
        "s_mov_b32 s46, 0x200000\n\t"
        "s_mov_b32 s47, 0x20000\n\t"
        "s_lshl_b32 s31, s24, 2\n\t"
        "s_add_u32 s67, s30, s31\n\t"
        "s_sub_u32 s67, s67, 16\n\t"
        "s_lshl_b32 s31, s27, 2\n\t"
        "s_add_u32 s31, s31, s24\n\t"
        "s_mul_i32 s31, s31, 20\n\t"
        "v_add_u32 v6, s31, v1\n\t"
        "v_lshlrev_b32 v6, 7, v6\n\t"
        "s_sub_u32 s33, s30, 16\n\t"
        "s_max_i32 s33, s33, 0\n\t"
        "s_lshl_b32 s33, s33, 10\n\t"
        "s_mov_b64 exec, 0xfffff\n\t"
        "buffer_load_dword v12, v6, s[44:47], s33 offen\n\t"
        "s_mov_b64 exec, -1\n\t"
        "s_add_i32 s68, s67, 0\n\t"
        "s_max_i32 s68, s68, 0\n\t"
        "s_lshl_b32 s68, s68, 10\n\t"
        "buffer_load_dwordx4 v[100:103], v2, s[44:47], s68 offen offset:0\n\t"
        "s_add_i32 s68, s67, 1\n\t"
        "s_max_i32 s68, s68, 0\n\t"
        "s_lshl_b32 s68, s68, 10\n\t"
        "buffer_load_dwordx4 v[108:111], v2, s[44:47], s68 offen offset:0\n\t"
        "s_add_i32 s68, s67, 2\n\t"
        "s_max_i32 s68, s68, 0\n\t"
        "s_lshl_b32 s68, s68, 10\n\t"
        "buffer_load_dwordx4 v[116:119], v2, s[44:47], s68 offen offset:0\n\t"
        "s_add_i32 s68, s67, 3\n\t"
        "s_max_i32 s68, s68, 0\n\t"
        "s_lshl_b32 s68, s68, 10\n\t"
        "buffer_load_dwordx4 v[124:127], v2, s[44:47], s68 offen offset:0\n\t"
        "s_mov_b32 s56, s20\n\t"
        "s_and_b32 s57, s21, 0xffff\n\t"
        "s_mov_b32 s58, 0x1000000\n\t"
        "s_mov_b32 s59, 0x20000\n\t"
        "s_mov_b32 s48, s6\n\t"
        "s_and_b32 s49, s7, 0xffff\n\t"
        "s_mov_b32 s50, 0x180000\n\t"
        "s_mov_b32 s51, 0x20000\n\t"
        "s_mul_i32 s69, s24, 393216\n\t"
        "s_lshl_b32 s31, s27, 7\n\t"
        "s_add_u32 s69, s69, s31\n\t"
        "s_lshl_b32 s70, s24, 14\n\t"
        "v_mul_u32_u24 v6, 3072, v11\n\t"
        "v_add_u32 v13, v6, v5\n\t"
        "v_add_u32 v18, v6, v10\n\t"
        "v_lshrrev_b32 v6, 2, v1\n\t"
        "v_and_b32 v6, 3, v6\n\t"
        "v_and_b32 v7, 3, v1\n\t"
        "v_lshl_add_u32 v8, v3, 3, v6\n\t"
        "v_lshlrev_b32 v5, 7, v8\n\t"
        "v_lshrrev_b32 v6, 1, v6\n\t"
        "v_and_b32 v8, 1, v3\n\t"
        "v_lshl_add_u32 v6, v8, 1, v6\n\t"
        "v_lshlrev_b32 v6, 1, v6\n\t"
        "v_lshrrev_b32 v8, 1, v7\n\t"
        "s_lshl_b32 s31, s24, 1\n\t"
        "v_add_u32 v8, s31, v8\n\t"
        "v_xor_b32 v8, v8, v6\n\t"
        "v_lshl_add_u32 v5, v8, 4, v5\n\t"
        "v_and_b32 v7, 1, v7\n\t"
        "v_lshl_add_u32 v5, v7, 3, v5\n\t"
        "v_add_u32 v10, 65536, v5\n\t"
        "v_add_u32 v11, 147456, v5\n\t"
        "s_lshl_b32 s71, s24, 8\n\t"
        "s_add_u32 s71, s71, %2\n\t"
        "s_cmp_lt_u32 s71, 128\n\t"
        "s_cbranch_scc1 .Lppwo1\n\t"
        "s_cmp_lt_u32 s71, 472\n\t"
        "s_cbranch_scc1 .Lppw22\n\t"
        "s_cmp_lt_u32 s71, 816\n\t"
        "s_cbranch_scc1 .Lppw13\n\t"
        "s_mov_b32 s60, s8\n\t"
        "s_and_b32 s61, s9, 0xffff\n\t"
        "s_mov_b32 s62, 0x0\n\t"
        "s_mov_b32 s63, 0x20000\n\t"
        "s_mov_b32 s74, 1024\n\t"
        "s_mov_b32 s75, 0\n\t"
        "s_mov_b32 s73, 0\n\t"
        "s_mov_b32 s72, 0x7f000000\n\t"
        "s_branch .Lppdone4\n\t"
        ".Lppwo1:\n\t"
        "s_mov_b32 s60, s8\n\t"
        "s_and_b32 s61, s9, 0xffff\n\t"
        "s_mov_b32 s62, 0x80000\n\t"
        "s_mov_b32 s63, 0x20000\n\t"
        "s_lshr_b32 s31, s71, 4\n\t"
        "s_and_b32 s33, s71, 15\n\t"
        "s_lshl_b32 s75, s33, 15\n\t"
        "s_lshl_b32 s31, s31, 7\n\t"
        "s_add_u32 s75, s75, s31\n\t"
        "s_lshl_b32 s72, s71, 12\n\t"
        "s_add_u32 s72, s72, 1048576\n\t"
        "s_mov_b32 s74, 1024\n\t"
        "s_mov_b32 s73, 0\n\t"
        "s_branch .Lppdone4\n\t"
        ".Lppw22:\n\t"
        "s_mov_b32 s60, s12\n\t"
        "s_and_b32 s61, s13, 0xffff\n\t"
        "s_mov_b32 s62, 0x158000\n\t"
        "s_mov_b32 s63, 0x20000\n\t"
        "s_sub_u32 s36, s71, 128\n\t"
        "s_mul_i32 s31, s36, 0xbe83\n\t"
        "s_lshr_b32 s31, s31, 21\n\t"
        "s_mul_i32 s33, s31, 43\n\t"
        "s_sub_u32 s33, s36, s33\n\t"
        "s_lshl_b32 s75, s33, 15\n\t"
        "s_lshl_b32 s31, s31, 7\n\t"
        "s_add_u32 s75, s75, s31\n\t"
        "s_lshl_b32 s72, s36, 12\n\t"
        "s_add_u32 s72, s72, 2981888\n\t"
        "s_mov_b32 s74, 1024\n\t"
        "s_mov_b32 s73, 0\n\t"
        "s_branch .Lppdone4\n\t"
        ".Lppw13:\n\t"
        "s_mov_b32 s60, s10\n\t"
        "s_and_b32 s61, s11, 0xffff\n\t"
        "s_mov_b32 s62, 0x158000\n\t"
        "s_mov_b32 s63, 0x20000\n\t"
        "s_sub_u32 s36, s71, 472\n\t"
        "s_lshr_b32 s31, s36, 3\n\t"
        "s_and_b32 s33, s36, 7\n\t"
        "s_mul_i32 s75, s33, 176128\n\t"
        "s_lshl_b32 s37, s31, 6\n\t"
        "s_add_u32 s75, s75, s37\n\t"
        "s_lshl_b32 s72, s31, 15\n\t"
        "s_lshl_b32 s33, s33, 11\n\t"
        "s_add_u32 s72, s72, s33\n\t"
        "s_add_u32 s72, s72, 1572864\n\t"
        "s_mov_b32 s74, 2752\n\t"
        "s_mov_b32 s73, 1\n\t"
        ".Lppdone4:\n\t"
        "s_mov_b32 s76, s18\n\t"
        "s_and_b32 s77, s19, 0xffff\n\t"
        "s_mov_b32 s78, 0x200000\n\t"
        "s_mov_b32 s79, 0x20000\n\t"
        "v_mov_b32 v20, 0\n\t"
        "v_mov_b32 v21, 0\n\t"
        "v_mov_b32 v22, 0\n\t"
        "v_mov_b32 v23, 0\n\t"
        "v_mov_b32 v24, 0\n\t"
        "v_mov_b32 v25, 0\n\t"
        "v_mov_b32 v26, 0\n\t"
        "v_mov_b32 v27, 0\n\t"
        "v_mov_b32 v28, 0\n\t"
        "v_mov_b32 v29, 0\n\t"
        "v_mov_b32 v30, 0\n\t"
        "v_mov_b32 v31, 0\n\t"
        "v_mov_b32 v32, 0\n\t"
        "v_mov_b32 v33, 0\n\t"
        "v_mov_b32 v34, 0\n\t"
        "v_mov_b32 v35, 0\n\t"
        "v_mov_b32 v36, 0\n\t"
        "v_mov_b32 v37, 0\n\t"
        "v_mov_b32 v38, 0\n\t"
        "v_mov_b32 v39, 0\n\t"
        "v_mov_b32 v40, 0\n\t"
        "v_mov_b32 v41, 0\n\t"
        "v_mov_b32 v42, 0\n\t"
        "v_mov_b32 v43, 0\n\t"
        "v_mov_b32 v44, 0\n\t"
        "v_mov_b32 v45, 0\n\t"
        "v_mov_b32 v46, 0\n\t"
        "v_mov_b32 v47, 0\n\t"
        "v_mov_b32 v48, 0\n\t"
        "v_mov_b32 v49, 0\n\t"
        "v_mov_b32 v50, 0\n\t"
        "v_mov_b32 v51, 0\n\t"
        "v_mov_b32 v52, 0\n\t"
        "v_mov_b32 v53, 0\n\t"
        "v_mov_b32 v54, 0\n\t"
        "v_mov_b32 v55, 0\n\t"
        "v_mov_b32 v56, 0\n\t"
        "v_mov_b32 v57, 0\n\t"
        "v_mov_b32 v58, 0\n\t"
        "v_mov_b32 v59, 0\n\t"
        "s_waitcnt vmcnt(10)\n\t"
        "v_lshlrev_b32 v14, 16, v14\n\t"
        "v_mul_f32 v6, 0xbfb8aa3b, v14\n\t"
        "v_exp_f32 v6, v6\n\t"
        "s_nop 0\n\t"
        "v_add_f32 v6, 1.0, v6\n\t"
        "v_rcp_f32 v7, v6\n\t"
        "s_nop 0\n\t"
        "v_fma_f32 v8, -v6, v7, 1.0\n\t"
        "v_fma_f32 v7, v8, v7, v7\n\t"
        "v_mul_f32 v14, v14, v7\n\t"
        "v_cvt_pk_bf16_f32 v14, v14, v14\n\t"
        "ds_write_b16 v15, v14\n\t"
        "s_add_i32 s68, s67, 16\n\t"
        "s_lshl_b32 s68, s68, 10\n\t"
        "buffer_load_dwordx4 v[132:135], v2, s[44:47], s68 offen offset:0\n\t"
        "buffer_load_dwordx4 v[140:143], v2, s[44:47], s68 offen offset:1024\n\t"
        "buffer_load_dwordx4 v[148:151], v2, s[44:47], s68 offen offset:2048\n\t"
        "buffer_load_dwordx4 v[156:159], v2, s[44:47], s68 offen offset:3072\n\t"
        "s_waitcnt vmcnt(10)\n\t"
        "s_waitcnt lgkmcnt(0)\n\t"
        "s_barrier\n\t"
        "v_lshlrev_b32 v6, 4, v3\n\t"
        "v_add_u32 v6, 131072, v6\n\t"
        "ds_read_b128 v[60:63], v6 offset:0\n\t"
        "ds_read_b64_tr_b16 v[76:77], v11 offset:0\n\t"
        "ds_read_b64_tr_b16 v[78:79], v11 offset:512\n\t"
        "ds_read_b128 v[64:67], v6 offset:64\n\t"
        "ds_read_b64_tr_b16 v[80:81], v11 offset:4096\n\t"
        "ds_read_b64_tr_b16 v[82:83], v11 offset:4608\n\t"
        "ds_read_b128 v[68:71], v6 offset:128\n\t"
        "ds_read_b64_tr_b16 v[84:85], v11 offset:8192\n\t"
        "ds_read_b64_tr_b16 v[86:87], v11 offset:8704\n\t"
        "ds_read_b128 v[72:75], v6 offset:192\n\t"
        "ds_read_b64_tr_b16 v[88:89], v11 offset:12288\n\t"
        "ds_read_b64_tr_b16 v[90:91], v11 offset:12800\n\t"
        "s_waitcnt vmcnt(9)\n\t"
        "v_readfirstlane_b32 s34, v16\n\t"
        "v_readfirstlane_b32 s35, v17\n\t"
        "v_lshrrev_b32 v7, 3, v1\n\t"
        "v_mul_u32_u24 v7, 49152, v7\n\t"
        "v_and_b32 v8, 7, v1\n\t"
        "v_lshl_add_u32 v7, v8, 3, v7\n\t"
        "v_lshlrev_b32 v8, 3, v8\n\t"
        "s_waitcnt lgkmcnt(9)\n\t"
        "v_mfma_f32_16x16x32_bf16 v[92:95], v[60:63], v[76:79], 0\n\t"
        "s_waitcnt lgkmcnt(6)\n\t"
        "v_mfma_f32_16x16x32_bf16 v[92:95], v[64:67], v[80:83], v[92:95]\n\t"
        "s_waitcnt lgkmcnt(3)\n\t"
        "v_mfma_f32_16x16x32_bf16 v[92:95], v[68:71], v[84:87], v[92:95]\n\t"
        "s_waitcnt lgkmcnt(0)\n\t"
        "v_mfma_f32_16x16x32_bf16 v[92:95], v[72:75], v[88:91], v[92:95]\n\t"
        "s_add_u32 s34, s34, 1\n\t"
        "s_addc_u32 s35, s35, 0\n\t"
        "s_mul_i32 s31, s65, 12288\n\t"
        "s_lshl_b32 s33, s64, 8\n\t"
        "s_add_u32 s31, s31, s33\n\t"
        "s_lshl_b32 s33, s24, 6\n\t"
        "s_add_u32 s31, s31, s33\n\t"
        "s_add_u32 s31, s31, 262144\n\t"
        "s_add_u32 s31, s31, s66\n\t"
        "v_mov_b32_dpp v93, v92 quad_perm:[1,0,3,2] row_mask:0xf bank_mask:0xf bound_ctrl:1\n\t"
        "v_cvt_pk_bf16_f32 v92, v92, v93\n\t"
        "ds_bpermute_b32 v94, v8, v92\n\t"
        "v_mov_b32 v95, s34\n\t"
        "s_waitcnt lgkmcnt(0)\n\t"
        "buffer_store_dwordx2 v[94:95], v7, s[56:59], s31 offen sc1\n\t"
        "v_mov_b32 v14, s34\n\t"
        "v_mov_b32 v15, s35\n\t"
        "s_add_i32 s68, s67, 32\n\t"
        "s_lshl_b32 s68, s68, 10\n\t"
        "buffer_load_dwordx4 v[164:167], v2, s[44:47], s68 offen offset:0\n\t"
        "buffer_load_dwordx4 v[172:175], v2, s[44:47], s68 offen offset:1024\n\t"
        "buffer_load_dwordx4 v[180:183], v2, s[44:47], s68 offen offset:2048\n\t"
        "buffer_load_dwordx4 v[188:191], v2, s[44:47], s68 offen offset:3072\n\t"
        "s_add_i32 s68, s67, 48\n\t"
        "s_lshl_b32 s68, s68, 10\n\t"
        "buffer_load_dwordx4 v[196:199], v2, s[44:47], s68 offen offset:0\n\t"
        "buffer_load_dwordx4 v[204:207], v2, s[44:47], s68 offen offset:1024\n\t"
        "buffer_load_dwordx4 v[212:215], v2, s[44:47], s68 offen offset:2048\n\t"
        "buffer_load_dwordx4 v[220:223], v2, s[44:47], s68 offen offset:3072\n\t"
        "s_add_i32 s68, s67, 64\n\t"
        "s_lshl_b32 s68, s68, 10\n\t"
        "buffer_load_dwordx4 v[228:231], v2, s[44:47], s68 offen offset:0\n\t"
        "buffer_load_dwordx4 v[236:239], v2, s[44:47], s68 offen offset:1024\n\t"
        "buffer_load_dwordx4 v[244:247], v2, s[44:47], s68 offen offset:2048\n\t"
        "buffer_load_dwordx4 v[252:255], v2, s[44:47], s68 offen offset:3072\n\t"
        "s_add_u32 s39, s28, 0\n\t"
        "s_and_b32 s39, s39, 3\n\t"
        "s_mul_i32 s37, s39, 98304\n\t"
        "s_lshl_b32 s38, s39, 12\n\t"
        "s_add_u32 s37, s37, s69\n\t"
        "s_add_u32 s38, s38, s70\n\t"
        "s_add_u32 m0, s38, 0\n\t"
        "s_add_u32 s37, s37, 1024\n\t"
        "buffer_load_dwordx4 v13, s[48:51], s37 offen lds\n\t"
        "s_add_u32 m0, m0, 1024\n\t"
        "s_add_u32 s37, s37, 24576\n\t"
        "buffer_load_dwordx4 v18, s[48:51], s37 offen lds\n\t"
        "s_add_u32 m0, m0, 1024\n\t"
        "s_add_u32 s37, s37, 24576\n\t"
        "buffer_load_dwordx4 v13, s[48:51], s37 offen lds\n\t"
        "s_add_u32 m0, m0, 1024\n\t"
        "s_add_u32 s37, s37, 24576\n\t"
        "buffer_load_dwordx4 v18, s[48:51], s37 offen lds\n\t"
        "s_add_u32 s39, s28, 1\n\t"
        "s_and_b32 s39, s39, 3\n\t"
        "s_mul_i32 s37, s39, 98304\n\t"
        "s_lshl_b32 s38, s39, 12\n\t"
        "s_add_u32 s37, s37, s69\n\t"
        "s_add_u32 s38, s38, s70\n\t"
        "s_add_u32 m0, s38, 0\n\t"
        "s_add_u32 s37, s37, 1024\n\t"
        "buffer_load_dwordx4 v13, s[48:51], s37 offen lds\n\t"
        "s_add_u32 m0, m0, 1024\n\t"
        "s_add_u32 s37, s37, 24576\n\t"
        "buffer_load_dwordx4 v18, s[48:51], s37 offen lds\n\t"
        "s_waitcnt vmcnt(26)\n\t"
        "v_dot2c_f32_bf16 v20, s43, v100\n\t"
        "v_dot2c_f32_bf16 v40, v100, v100\n\t"
        "v_dot2c_f32_bf16 v20, s43, v101\n\t"
        "v_dot2c_f32_bf16 v40, v101, v101\n\t"
        "v_dot2c_f32_bf16 v20, s43, v102\n\t"
        "v_dot2c_f32_bf16 v40, v102, v102\n\t"
        "v_dot2c_f32_bf16 v20, s43, v103\n\t"
        "v_dot2c_f32_bf16 v40, v103, v103\n\t"
        "v_lshlrev_b32 v96, 16, v100\n\t"
        "v_and_b32 v97, s42, v100\n\t"
        "v_lshlrev_b32 v98, 16, v101\n\t"
        "v_and_b32 v99, s42, v101\n\t"
        "v_lshlrev_b32 v100, 16, v102\n\t"
        "v_and_b32 v101, s42, v102\n\t"
        "v_lshlrev_b32 v102, 16, v103\n\t"
        "v_and_b32 v103, s42, v103\n\t"
        "s_add_u32 m0, m0, 1024\n\t"
        "s_add_u32 s37, s37, 24576\n\t"
        "buffer_load_dwordx4 v13, s[48:51], s37 offen lds\n\t"
        "s_waitcnt vmcnt(26)\n\t"
        "v_dot2c_f32_bf16 v21, s43, v108\n\t"
        "v_dot2c_f32_bf16 v41, v108, v108\n\t"
        "v_dot2c_f32_bf16 v21, s43, v109\n\t"
        "v_dot2c_f32_bf16 v41, v109, v109\n\t"
        "v_dot2c_f32_bf16 v21, s43, v110\n\t"
        "v_dot2c_f32_bf16 v41, v110, v110\n\t"
        "v_dot2c_f32_bf16 v21, s43, v111\n\t"
        "v_dot2c_f32_bf16 v41, v111, v111\n\t"
        "v_lshlrev_b32 v104, 16, v108\n\t"
        "v_and_b32 v105, s42, v108\n\t"
        "v_lshlrev_b32 v106, 16, v109\n\t"
        "v_and_b32 v107, s42, v109\n\t"
        "v_lshlrev_b32 v108, 16, v110\n\t"
        "v_and_b32 v109, s42, v110\n\t"
        "v_lshlrev_b32 v110, 16, v111\n\t"
        "v_and_b32 v111, s42, v111\n\t"
        "s_add_u32 m0, m0, 1024\n\t"
        "s_add_u32 s37, s37, 24576\n\t"
        "buffer_load_dwordx4 v18, s[48:51], s37 offen lds\n\t"
        "s_waitcnt vmcnt(26)\n\t"
        "v_dot2c_f32_bf16 v22, s43, v116\n\t"
        "v_dot2c_f32_bf16 v42, v116, v116\n\t"
        "v_dot2c_f32_bf16 v22, s43, v117\n\t"
        "v_dot2c_f32_bf16 v42, v117, v117\n\t"
        "v_dot2c_f32_bf16 v22, s43, v118\n\t"
        "v_dot2c_f32_bf16 v42, v118, v118\n\t"
        "v_dot2c_f32_bf16 v22, s43, v119\n\t"
        "v_dot2c_f32_bf16 v42, v119, v119\n\t"
        "v_lshlrev_b32 v112, 16, v116\n\t"
        "v_and_b32 v113, s42, v116\n\t"
        "v_lshlrev_b32 v114, 16, v117\n\t"
        "v_and_b32 v115, s42, v117\n\t"
        "v_lshlrev_b32 v116, 16, v118\n\t"
        "v_and_b32 v117, s42, v118\n\t"
        "v_lshlrev_b32 v118, 16, v119\n\t"
        "v_and_b32 v119, s42, v119\n\t"
        "s_add_u32 s39, s28, 2\n\t"
        "s_and_b32 s39, s39, 3\n\t"
        "s_mul_i32 s37, s39, 98304\n\t"
        "s_lshl_b32 s38, s39, 12\n\t"
        "s_add_u32 s37, s37, s69\n\t"
        "s_add_u32 s38, s38, s70\n\t"
        "s_add_u32 m0, s38, 0\n\t"
        "s_add_u32 s37, s37, 1024\n\t"
        "buffer_load_dwordx4 v13, s[48:51], s37 offen lds\n\t"
        "s_waitcnt vmcnt(26)\n\t"
        "v_dot2c_f32_bf16 v23, s43, v124\n\t"
        "v_dot2c_f32_bf16 v43, v124, v124\n\t"
        "v_dot2c_f32_bf16 v23, s43, v125\n\t"
        "v_dot2c_f32_bf16 v43, v125, v125\n\t"
        "v_dot2c_f32_bf16 v23, s43, v126\n\t"
        "v_dot2c_f32_bf16 v43, v126, v126\n\t"
        "v_dot2c_f32_bf16 v23, s43, v127\n\t"
        "v_dot2c_f32_bf16 v43, v127, v127\n\t"
        "v_lshlrev_b32 v120, 16, v124\n\t"
        "v_and_b32 v121, s42, v124\n\t"
        "v_lshlrev_b32 v122, 16, v125\n\t"
        "v_and_b32 v123, s42, v125\n\t"
        "v_lshlrev_b32 v124, 16, v126\n\t"
        "v_and_b32 v125, s42, v126\n\t"
        "v_lshlrev_b32 v126, 16, v127\n\t"
        "v_and_b32 v127, s42, v127\n\t"
        "s_add_u32 m0, m0, 1024\n\t"
        "s_add_u32 s37, s37, 24576\n\t"
        "buffer_load_dwordx4 v18, s[48:51], s37 offen lds\n\t"
        "s_waitcnt vmcnt(26)\n\t"
        "v_dot2c_f32_bf16 v24, s43, v132\n\t"
        "v_dot2c_f32_bf16 v44, v132, v132\n\t"
        "v_dot2c_f32_bf16 v24, s43, v133\n\t"
        "v_dot2c_f32_bf16 v44, v133, v133\n\t"
        "v_dot2c_f32_bf16 v24, s43, v134\n\t"
        "v_dot2c_f32_bf16 v44, v134, v134\n\t"
        "v_dot2c_f32_bf16 v24, s43, v135\n\t"
        "v_dot2c_f32_bf16 v44, v135, v135\n\t"
        "v_lshlrev_b32 v128, 16, v132\n\t"
        "v_and_b32 v129, s42, v132\n\t"
        "v_lshlrev_b32 v130, 16, v133\n\t"
        "v_and_b32 v131, s42, v133\n\t"
        "v_lshlrev_b32 v132, 16, v134\n\t"
        "v_and_b32 v133, s42, v134\n\t"
        "v_lshlrev_b32 v134, 16, v135\n\t"
        "v_and_b32 v135, s42, v135\n\t"
        "s_add_u32 m0, m0, 1024\n\t"
        "s_add_u32 s37, s37, 24576\n\t"
        "buffer_load_dwordx4 v13, s[48:51], s37 offen lds\n\t"
        "s_waitcnt vmcnt(26)\n\t"
        "v_dot2c_f32_bf16 v25, s43, v140\n\t"
        "v_dot2c_f32_bf16 v45, v140, v140\n\t"
        "v_dot2c_f32_bf16 v25, s43, v141\n\t"
        "v_dot2c_f32_bf16 v45, v141, v141\n\t"
        "v_dot2c_f32_bf16 v25, s43, v142\n\t"
        "v_dot2c_f32_bf16 v45, v142, v142\n\t"
        "v_dot2c_f32_bf16 v25, s43, v143\n\t"
        "v_dot2c_f32_bf16 v45, v143, v143\n\t"
        "v_lshlrev_b32 v136, 16, v140\n\t"
        "v_and_b32 v137, s42, v140\n\t"
        "v_lshlrev_b32 v138, 16, v141\n\t"
        "v_and_b32 v139, s42, v141\n\t"
        "v_lshlrev_b32 v140, 16, v142\n\t"
        "v_and_b32 v141, s42, v142\n\t"
        "v_lshlrev_b32 v142, 16, v143\n\t"
        "v_and_b32 v143, s42, v143\n\t"
        "s_add_u32 m0, m0, 1024\n\t"
        "s_add_u32 s37, s37, 24576\n\t"
        "buffer_load_dwordx4 v18, s[48:51], s37 offen lds\n\t"
        "s_waitcnt vmcnt(26)\n\t"
        "v_dot2c_f32_bf16 v26, s43, v148\n\t"
        "v_dot2c_f32_bf16 v46, v148, v148\n\t"
        "v_dot2c_f32_bf16 v26, s43, v149\n\t"
        "v_dot2c_f32_bf16 v46, v149, v149\n\t"
        "v_dot2c_f32_bf16 v26, s43, v150\n\t"
        "v_dot2c_f32_bf16 v46, v150, v150\n\t"
        "v_dot2c_f32_bf16 v26, s43, v151\n\t"
        "v_dot2c_f32_bf16 v46, v151, v151\n\t"
        "v_lshlrev_b32 v144, 16, v148\n\t"
        "v_and_b32 v145, s42, v148\n\t"
        "v_lshlrev_b32 v146, 16, v149\n\t"
        "v_and_b32 v147, s42, v149\n\t"
        "v_lshlrev_b32 v148, 16, v150\n\t"
        "v_and_b32 v149, s42, v150\n\t"
        "v_lshlrev_b32 v150, 16, v151\n\t"
        "v_and_b32 v151, s42, v151\n\t"
        "s_add_u32 s39, s28, 3\n\t"
        "s_and_b32 s39, s39, 3\n\t"
        "s_mul_i32 s37, s39, 98304\n\t"
        "s_lshl_b32 s38, s39, 12\n\t"
        "s_add_u32 s37, s37, s69\n\t"
        "s_add_u32 s38, s38, s70\n\t"
        "s_add_u32 m0, s38, 0\n\t"
        "s_add_u32 s37, s37, 1024\n\t"
        "buffer_load_dwordx4 v13, s[48:51], s37 offen lds\n\t"
        "s_waitcnt vmcnt(26)\n\t"
        "v_dot2c_f32_bf16 v27, s43, v156\n\t"
        "v_dot2c_f32_bf16 v47, v156, v156\n\t"
        "v_dot2c_f32_bf16 v27, s43, v157\n\t"
        "v_dot2c_f32_bf16 v47, v157, v157\n\t"
        "v_dot2c_f32_bf16 v27, s43, v158\n\t"
        "v_dot2c_f32_bf16 v47, v158, v158\n\t"
        "v_dot2c_f32_bf16 v27, s43, v159\n\t"
        "v_dot2c_f32_bf16 v47, v159, v159\n\t"
        "v_lshlrev_b32 v152, 16, v156\n\t"
        "v_and_b32 v153, s42, v156\n\t"
        "v_lshlrev_b32 v154, 16, v157\n\t"
        "v_and_b32 v155, s42, v157\n\t"
        "v_lshlrev_b32 v156, 16, v158\n\t"
        "v_and_b32 v157, s42, v158\n\t"
        "v_lshlrev_b32 v158, 16, v159\n\t"
        "v_and_b32 v159, s42, v159\n\t"
        "s_add_u32 m0, m0, 1024\n\t"
        "s_add_u32 s37, s37, 24576\n\t"
        "buffer_load_dwordx4 v18, s[48:51], s37 offen lds\n\t"
        "s_waitcnt vmcnt(25)\n\t"
        "v_dot2c_f32_bf16 v28, s43, v164\n\t"
        "v_dot2c_f32_bf16 v48, v164, v164\n\t"
        "v_dot2c_f32_bf16 v28, s43, v165\n\t"
        "v_dot2c_f32_bf16 v48, v165, v165\n\t"
        "v_dot2c_f32_bf16 v28, s43, v166\n\t"
        "v_dot2c_f32_bf16 v48, v166, v166\n\t"
        "v_dot2c_f32_bf16 v28, s43, v167\n\t"
        "v_dot2c_f32_bf16 v48, v167, v167\n\t"
        "v_lshlrev_b32 v160, 16, v164\n\t"
        "v_and_b32 v161, s42, v164\n\t"
        "v_lshlrev_b32 v162, 16, v165\n\t"
        "v_and_b32 v163, s42, v165\n\t"
        "v_lshlrev_b32 v164, 16, v166\n\t"
        "v_and_b32 v165, s42, v166\n\t"
        "v_lshlrev_b32 v166, 16, v167\n\t"
        "v_and_b32 v167, s42, v167\n\t"
        "s_add_u32 m0, m0, 1024\n\t"
        "s_add_u32 s37, s37, 24576\n\t"
        "buffer_load_dwordx4 v13, s[48:51], s37 offen lds\n\t"
        "s_waitcnt vmcnt(25)\n\t"
        "v_dot2c_f32_bf16 v29, s43, v172\n\t"
        "v_dot2c_f32_bf16 v49, v172, v172\n\t"
        "v_dot2c_f32_bf16 v29, s43, v173\n\t"
        "v_dot2c_f32_bf16 v49, v173, v173\n\t"
        "v_dot2c_f32_bf16 v29, s43, v174\n\t"
        "v_dot2c_f32_bf16 v49, v174, v174\n\t"
        "v_dot2c_f32_bf16 v29, s43, v175\n\t"
        "v_dot2c_f32_bf16 v49, v175, v175\n\t"
        "v_lshlrev_b32 v168, 16, v172\n\t"
        "v_and_b32 v169, s42, v172\n\t"
        "v_lshlrev_b32 v170, 16, v173\n\t"
        "v_and_b32 v171, s42, v173\n\t"
        "v_lshlrev_b32 v172, 16, v174\n\t"
        "v_and_b32 v173, s42, v174\n\t"
        "v_lshlrev_b32 v174, 16, v175\n\t"
        "v_and_b32 v175, s42, v175\n\t"
        "s_add_u32 m0, m0, 1024\n\t"
        "s_add_u32 s37, s37, 24576\n\t"
        "buffer_load_dwordx4 v18, s[48:51], s37 offen lds\n\t"
        "s_waitcnt vmcnt(25)\n\t"
        "v_dot2c_f32_bf16 v30, s43, v180\n\t"
        "v_dot2c_f32_bf16 v50, v180, v180\n\t"
        "v_dot2c_f32_bf16 v30, s43, v181\n\t"
        "v_dot2c_f32_bf16 v50, v181, v181\n\t"
        "v_dot2c_f32_bf16 v30, s43, v182\n\t"
        "v_dot2c_f32_bf16 v50, v182, v182\n\t"
        "v_dot2c_f32_bf16 v30, s43, v183\n\t"
        "v_dot2c_f32_bf16 v50, v183, v183\n\t"
        "v_lshlrev_b32 v176, 16, v180\n\t"
        "v_and_b32 v177, s42, v180\n\t"
        "v_lshlrev_b32 v178, 16, v181\n\t"
        "v_and_b32 v179, s42, v181\n\t"
        "v_lshlrev_b32 v180, 16, v182\n\t"
        "v_and_b32 v181, s42, v182\n\t"
        "v_lshlrev_b32 v182, 16, v183\n\t"
        "v_and_b32 v183, s42, v183\n\t"
        "s_add_u32 s39, s28, 0\n\t"
        "s_and_b32 s39, s39, 3\n\t"
        "s_mul_i32 s37, s39, 98304\n\t"
        "s_lshl_b32 s38, s39, 12\n\t"
        "s_add_u32 s37, s37, s69\n\t"
        "s_add_u32 s38, s38, s70\n\t"
        "s_add_u32 m0, s38, 65536\n\t"
        "s_add_u32 s37, s37, 2048\n\t"
        "buffer_load_dwordx4 v13, s[48:51], s37 offen lds\n\t"
        "s_waitcnt vmcnt(25)\n\t"
        "v_dot2c_f32_bf16 v31, s43, v188\n\t"
        "v_dot2c_f32_bf16 v51, v188, v188\n\t"
        "v_dot2c_f32_bf16 v31, s43, v189\n\t"
        "v_dot2c_f32_bf16 v51, v189, v189\n\t"
        "v_dot2c_f32_bf16 v31, s43, v190\n\t"
        "v_dot2c_f32_bf16 v51, v190, v190\n\t"
        "v_dot2c_f32_bf16 v31, s43, v191\n\t"
        "v_dot2c_f32_bf16 v51, v191, v191\n\t"
        "v_lshlrev_b32 v184, 16, v188\n\t"
        "v_and_b32 v185, s42, v188\n\t"
        "v_lshlrev_b32 v186, 16, v189\n\t"
        "v_and_b32 v187, s42, v189\n\t"
        "v_lshlrev_b32 v188, 16, v190\n\t"
        "v_and_b32 v189, s42, v190\n\t"
        "v_lshlrev_b32 v190, 16, v191\n\t"
        "v_and_b32 v191, s42, v191\n\t"
        "s_add_u32 m0, m0, 1024\n\t"
        "s_add_u32 s37, s37, 24576\n\t"
        "buffer_load_dwordx4 v18, s[48:51], s37 offen lds\n\t"
        "s_waitcnt vmcnt(25)\n\t"
        "v_dot2c_f32_bf16 v32, s43, v196\n\t"
        "v_dot2c_f32_bf16 v52, v196, v196\n\t"
        "v_dot2c_f32_bf16 v32, s43, v197\n\t"
        "v_dot2c_f32_bf16 v52, v197, v197\n\t"
        "v_dot2c_f32_bf16 v32, s43, v198\n\t"
        "v_dot2c_f32_bf16 v52, v198, v198\n\t"
        "v_dot2c_f32_bf16 v32, s43, v199\n\t"
        "v_dot2c_f32_bf16 v52, v199, v199\n\t"
        "v_lshlrev_b32 v192, 16, v196\n\t"
        "v_and_b32 v193, s42, v196\n\t"
        "v_lshlrev_b32 v194, 16, v197\n\t"
        "v_and_b32 v195, s42, v197\n\t"
        "v_lshlrev_b32 v196, 16, v198\n\t"
        "v_and_b32 v197, s42, v198\n\t"
        "v_lshlrev_b32 v198, 16, v199\n\t"
        "v_and_b32 v199, s42, v199\n\t"
        "s_add_u32 m0, m0, 1024\n\t"
        "s_add_u32 s37, s37, 24576\n\t"
        "buffer_load_dwordx4 v13, s[48:51], s37 offen lds\n\t"
        "s_waitcnt vmcnt(25)\n\t"
        "v_dot2c_f32_bf16 v33, s43, v204\n\t"
        "v_dot2c_f32_bf16 v53, v204, v204\n\t"
        "v_dot2c_f32_bf16 v33, s43, v205\n\t"
        "v_dot2c_f32_bf16 v53, v205, v205\n\t"
        "v_dot2c_f32_bf16 v33, s43, v206\n\t"
        "v_dot2c_f32_bf16 v53, v206, v206\n\t"
        "v_dot2c_f32_bf16 v33, s43, v207\n\t"
        "v_dot2c_f32_bf16 v53, v207, v207\n\t"
        "v_lshlrev_b32 v200, 16, v204\n\t"
        "v_and_b32 v201, s42, v204\n\t"
        "v_lshlrev_b32 v202, 16, v205\n\t"
        "v_and_b32 v203, s42, v205\n\t"
        "v_lshlrev_b32 v204, 16, v206\n\t"
        "v_and_b32 v205, s42, v206\n\t"
        "v_lshlrev_b32 v206, 16, v207\n\t"
        "v_and_b32 v207, s42, v207\n\t"
        "s_add_u32 m0, m0, 1024\n\t"
        "s_add_u32 s37, s37, 24576\n\t"
        "buffer_load_dwordx4 v18, s[48:51], s37 offen lds\n\t"
        "s_waitcnt vmcnt(25)\n\t"
        "v_dot2c_f32_bf16 v34, s43, v212\n\t"
        "v_dot2c_f32_bf16 v54, v212, v212\n\t"
        "v_dot2c_f32_bf16 v34, s43, v213\n\t"
        "v_dot2c_f32_bf16 v54, v213, v213\n\t"
        "v_dot2c_f32_bf16 v34, s43, v214\n\t"
        "v_dot2c_f32_bf16 v54, v214, v214\n\t"
        "v_dot2c_f32_bf16 v34, s43, v215\n\t"
        "v_dot2c_f32_bf16 v54, v215, v215\n\t"
        "v_lshlrev_b32 v208, 16, v212\n\t"
        "v_and_b32 v209, s42, v212\n\t"
        "v_lshlrev_b32 v210, 16, v213\n\t"
        "v_and_b32 v211, s42, v213\n\t"
        "v_lshlrev_b32 v212, 16, v214\n\t"
        "v_and_b32 v213, s42, v214\n\t"
        "v_lshlrev_b32 v214, 16, v215\n\t"
        "v_and_b32 v215, s42, v215\n\t"
        "s_add_u32 s39, s28, 1\n\t"
        "s_and_b32 s39, s39, 3\n\t"
        "s_mul_i32 s37, s39, 98304\n\t"
        "s_lshl_b32 s38, s39, 12\n\t"
        "s_add_u32 s37, s37, s69\n\t"
        "s_add_u32 s38, s38, s70\n\t"
        "s_add_u32 m0, s38, 65536\n\t"
        "s_add_u32 s37, s37, 2048\n\t"
        "buffer_load_dwordx4 v13, s[48:51], s37 offen lds\n\t"
        "s_waitcnt vmcnt(25)\n\t"
        "v_dot2c_f32_bf16 v35, s43, v220\n\t"
        "v_dot2c_f32_bf16 v55, v220, v220\n\t"
        "v_dot2c_f32_bf16 v35, s43, v221\n\t"
        "v_dot2c_f32_bf16 v55, v221, v221\n\t"
        "v_dot2c_f32_bf16 v35, s43, v222\n\t"
        "v_dot2c_f32_bf16 v55, v222, v222\n\t"
        "v_dot2c_f32_bf16 v35, s43, v223\n\t"
        "v_dot2c_f32_bf16 v55, v223, v223\n\t"
        "v_lshlrev_b32 v216, 16, v220\n\t"
        "v_and_b32 v217, s42, v220\n\t"
        "v_lshlrev_b32 v218, 16, v221\n\t"
        "v_and_b32 v219, s42, v221\n\t"
        "v_lshlrev_b32 v220, 16, v222\n\t"
        "v_and_b32 v221, s42, v222\n\t"
        "v_lshlrev_b32 v222, 16, v223\n\t"
        "v_and_b32 v223, s42, v223\n\t"
        "s_add_u32 m0, m0, 1024\n\t"
        "s_add_u32 s37, s37, 24576\n\t"
        "buffer_load_dwordx4 v18, s[48:51], s37 offen lds\n\t"
        "s_waitcnt vmcnt(25)\n\t"
        "v_dot2c_f32_bf16 v36, s43, v228\n\t"
        "v_dot2c_f32_bf16 v56, v228, v228\n\t"
        "v_dot2c_f32_bf16 v36, s43, v229\n\t"
        "v_dot2c_f32_bf16 v56, v229, v229\n\t"
        "v_dot2c_f32_bf16 v36, s43, v230\n\t"
        "v_dot2c_f32_bf16 v56, v230, v230\n\t"
        "v_dot2c_f32_bf16 v36, s43, v231\n\t"
        "v_dot2c_f32_bf16 v56, v231, v231\n\t"
        "v_lshlrev_b32 v224, 16, v228\n\t"
        "v_and_b32 v225, s42, v228\n\t"
        "v_lshlrev_b32 v226, 16, v229\n\t"
        "v_and_b32 v227, s42, v229\n\t"
        "v_lshlrev_b32 v228, 16, v230\n\t"
        "v_and_b32 v229, s42, v230\n\t"
        "v_lshlrev_b32 v230, 16, v231\n\t"
        "v_and_b32 v231, s42, v231\n\t"
        "s_add_u32 m0, m0, 1024\n\t"
        "s_add_u32 s37, s37, 24576\n\t"
        "buffer_load_dwordx4 v13, s[48:51], s37 offen lds\n\t"
        "s_waitcnt vmcnt(25)\n\t"
        "v_dot2c_f32_bf16 v37, s43, v236\n\t"
        "v_dot2c_f32_bf16 v57, v236, v236\n\t"
        "v_dot2c_f32_bf16 v37, s43, v237\n\t"
        "v_dot2c_f32_bf16 v57, v237, v237\n\t"
        "v_dot2c_f32_bf16 v37, s43, v238\n\t"
        "v_dot2c_f32_bf16 v57, v238, v238\n\t"
        "v_dot2c_f32_bf16 v37, s43, v239\n\t"
        "v_dot2c_f32_bf16 v57, v239, v239\n\t"
        "v_lshlrev_b32 v232, 16, v236\n\t"
        "v_and_b32 v233, s42, v236\n\t"
        "v_lshlrev_b32 v234, 16, v237\n\t"
        "v_and_b32 v235, s42, v237\n\t"
        "v_lshlrev_b32 v236, 16, v238\n\t"
        "v_and_b32 v237, s42, v238\n\t"
        "v_lshlrev_b32 v238, 16, v239\n\t"
        "v_and_b32 v239, s42, v239\n\t"
        "s_add_u32 m0, m0, 1024\n\t"
        "s_add_u32 s37, s37, 24576\n\t"
        "buffer_load_dwordx4 v18, s[48:51], s37 offen lds\n\t"
        "s_waitcnt vmcnt(25)\n\t"
        "v_dot2c_f32_bf16 v38, s43, v244\n\t"
        "v_dot2c_f32_bf16 v58, v244, v244\n\t"
        "v_dot2c_f32_bf16 v38, s43, v245\n\t"
        "v_dot2c_f32_bf16 v58, v245, v245\n\t"
        "v_dot2c_f32_bf16 v38, s43, v246\n\t"
        "v_dot2c_f32_bf16 v58, v246, v246\n\t"
        "v_dot2c_f32_bf16 v38, s43, v247\n\t"
        "v_dot2c_f32_bf16 v58, v247, v247\n\t"
        "v_lshlrev_b32 v240, 16, v244\n\t"
        "v_and_b32 v241, s42, v244\n\t"
        "v_lshlrev_b32 v242, 16, v245\n\t"
        "v_and_b32 v243, s42, v245\n\t"
        "v_lshlrev_b32 v244, 16, v246\n\t"
        "v_and_b32 v245, s42, v246\n\t"
        "v_lshlrev_b32 v246, 16, v247\n\t"
        "v_and_b32 v247, s42, v247\n\t"
        "s_add_u32 s39, s28, 2\n\t"
        "s_and_b32 s39, s39, 3\n\t"
        "s_mul_i32 s37, s39, 98304\n\t"
        "s_lshl_b32 s38, s39, 12\n\t"
        "s_add_u32 s37, s37, s69\n\t"
        "s_add_u32 s38, s38, s70\n\t"
        "s_add_u32 m0, s38, 65536\n\t"
        "s_add_u32 s37, s37, 2048\n\t"
        "buffer_load_dwordx4 v13, s[48:51], s37 offen lds\n\t"
        "s_waitcnt vmcnt(25)\n\t"
        "v_dot2c_f32_bf16 v39, s43, v252\n\t"
        "v_dot2c_f32_bf16 v59, v252, v252\n\t"
        "v_dot2c_f32_bf16 v39, s43, v253\n\t"
        "v_dot2c_f32_bf16 v59, v253, v253\n\t"
        "v_dot2c_f32_bf16 v39, s43, v254\n\t"
        "v_dot2c_f32_bf16 v59, v254, v254\n\t"
        "v_dot2c_f32_bf16 v39, s43, v255\n\t"
        "v_dot2c_f32_bf16 v59, v255, v255\n\t"
        "v_lshlrev_b32 v248, 16, v252\n\t"
        "v_and_b32 v249, s42, v252\n\t"
        "v_lshlrev_b32 v250, 16, v253\n\t"
        "v_and_b32 v251, s42, v253\n\t"
        "v_lshlrev_b32 v252, 16, v254\n\t"
        "v_and_b32 v253, s42, v254\n\t"
        "v_lshlrev_b32 v254, 16, v255\n\t"
        "v_and_b32 v255, s42, v255\n\t"
        "s_add_u32 m0, m0, 1024\n\t"
        "s_add_u32 s37, s37, 24576\n\t"
        "buffer_load_dwordx4 v18, s[48:51], s37 offen lds\n\t"
        "v_permlane32_swap_b32 v20, v30\n\t"
        "v_add_f32 v20, v20, v30\n\t"
        "v_permlane32_swap_b32 v40, v50\n\t"
        "v_add_f32 v40, v40, v50\n\t"
        "v_permlane32_swap_b32 v21, v31\n\t"
        "v_add_f32 v21, v21, v31\n\t"
        "s_add_u32 m0, m0, 1024\n\t"
        "s_add_u32 s37, s37, 24576\n\t"
        "buffer_load_dwordx4 v13, s[48:51], s37 offen lds\n\t"
        "v_permlane32_swap_b32 v41, v51\n\t"
        "v_add_f32 v41, v41, v51\n\t"
        "v_permlane32_swap_b32 v22, v32\n\t"
        "v_add_f32 v22, v22, v32\n\t"
        "v_permlane32_swap_b32 v42, v52\n\t"
        "v_add_f32 v42, v42, v52\n\t"
        "s_add_u32 m0, m0, 1024\n\t"
        "s_add_u32 s37, s37, 24576\n\t"
        "buffer_load_dwordx4 v18, s[48:51], s37 offen lds\n\t"
        "v_permlane32_swap_b32 v23, v33\n\t"
        "v_add_f32 v23, v23, v33\n\t"
        "v_permlane32_swap_b32 v43, v53\n\t"
        "v_add_f32 v43, v43, v53\n\t"
        "v_permlane32_swap_b32 v24, v34\n\t"
        "v_add_f32 v24, v24, v34\n\t"
        "s_add_u32 s39, s28, 3\n\t"
        "s_and_b32 s39, s39, 3\n\t"
        "s_mul_i32 s37, s39, 98304\n\t"
        "s_lshl_b32 s38, s39, 12\n\t"
        "s_add_u32 s37, s37, s69\n\t"
        "s_add_u32 s38, s38, s70\n\t"
        "s_add_u32 m0, s38, 65536\n\t"
        "s_add_u32 s37, s37, 2048\n\t"
        "buffer_load_dwordx4 v13, s[48:51], s37 offen lds\n\t"
        "v_permlane32_swap_b32 v44, v54\n\t"
        "v_add_f32 v44, v44, v54\n\t"
        "v_permlane32_swap_b32 v25, v35\n\t"
        "v_add_f32 v25, v25, v35\n\t"
        "v_permlane32_swap_b32 v45, v55\n\t"
        "v_add_f32 v45, v45, v55\n\t"
        "s_add_u32 m0, m0, 1024\n\t"
        "s_add_u32 s37, s37, 24576\n\t"
        "buffer_load_dwordx4 v18, s[48:51], s37 offen lds\n\t"
        "v_permlane32_swap_b32 v26, v36\n\t"
        "v_add_f32 v26, v26, v36\n\t"
        "v_permlane32_swap_b32 v46, v56\n\t"
        "v_add_f32 v46, v46, v56\n\t"
        "v_permlane32_swap_b32 v27, v37\n\t"
        "v_add_f32 v27, v27, v37\n\t"
        "s_add_u32 m0, m0, 1024\n\t"
        "s_add_u32 s37, s37, 24576\n\t"
        "buffer_load_dwordx4 v13, s[48:51], s37 offen lds\n\t"
        "v_permlane32_swap_b32 v47, v57\n\t"
        "v_add_f32 v47, v47, v57\n\t"
        "v_permlane32_swap_b32 v28, v38\n\t"
        "v_add_f32 v28, v28, v38\n\t"
        "v_permlane32_swap_b32 v48, v58\n\t"
        "v_add_f32 v48, v48, v58\n\t"
        "s_add_u32 m0, m0, 1024\n\t"
        "s_add_u32 s37, s37, 24576\n\t"
        "buffer_load_dwordx4 v18, s[48:51], s37 offen lds\n\t"
        "v_permlane32_swap_b32 v29, v39\n\t"
        "v_add_f32 v29, v29, v39\n\t"
        "v_permlane32_swap_b32 v49, v59\n\t"
        "v_add_f32 v49, v49, v59\n\t"
        "v_permlane16_swap_b32 v20, v25\n\t"
        "v_add_f32 v20, v20, v25\n\t"
        "v_permlane16_swap_b32 v40, v45\n\t"
        "v_add_f32 v40, v40, v45\n\t"
        "v_permlane16_swap_b32 v21, v26\n\t"
        "v_add_f32 v21, v21, v26\n\t"
        "v_permlane16_swap_b32 v41, v46\n\t"
        "v_add_f32 v41, v41, v46\n\t"
        "v_permlane16_swap_b32 v22, v27\n\t"
        "v_add_f32 v22, v22, v27\n\t"
        "v_permlane16_swap_b32 v42, v47\n\t"
        "v_add_f32 v42, v42, v47\n\t"
        "v_permlane16_swap_b32 v23, v28\n\t"
        "v_add_f32 v23, v23, v28\n\t"
        "v_permlane16_swap_b32 v43, v48\n\t"
        "v_add_f32 v43, v43, v48\n\t"
        "v_permlane16_swap_b32 v24, v29\n\t"
        "v_add_f32 v24, v24, v29\n\t"
        "v_permlane16_swap_b32 v44, v49\n\t"
        "v_add_f32 v44, v44, v49\n\t"
        "v_add_f32_dpp v20, v20, v20 row_ror:8 row_mask:0xf bank_mask:0xf\n\t"
        "v_add_f32_dpp v21, v21, v21 row_ror:8 row_mask:0xf bank_mask:0xf\n\t"
        "v_add_f32_dpp v22, v22, v22 row_ror:8 row_mask:0xf bank_mask:0xf\n\t"
        "v_add_f32_dpp v23, v23, v23 row_ror:8 row_mask:0xf bank_mask:0xf\n\t"
        "v_add_f32_dpp v24, v24, v24 row_ror:8 row_mask:0xf bank_mask:0xf\n\t"
        "v_add_f32_dpp v40, v40, v40 row_ror:8 row_mask:0xf bank_mask:0xf\n\t"
        "v_add_f32_dpp v41, v41, v41 row_ror:8 row_mask:0xf bank_mask:0xf\n\t"
        "v_add_f32_dpp v42, v42, v42 row_ror:8 row_mask:0xf bank_mask:0xf\n\t"
        "v_add_f32_dpp v43, v43, v43 row_ror:8 row_mask:0xf bank_mask:0xf\n\t"
        "v_add_f32_dpp v44, v44, v44 row_ror:8 row_mask:0xf bank_mask:0xf\n\t"
        "v_add_f32_dpp v20, v20, v20 row_ror:4 row_mask:0xf bank_mask:0xf\n\t"
        "v_add_f32_dpp v21, v21, v21 row_ror:4 row_mask:0xf bank_mask:0xf\n\t"
        "v_add_f32_dpp v22, v22, v22 row_ror:4 row_mask:0xf bank_mask:0xf\n\t"
        "v_add_f32_dpp v23, v23, v23 row_ror:4 row_mask:0xf bank_mask:0xf\n\t"
        "v_add_f32_dpp v24, v24, v24 row_ror:4 row_mask:0xf bank_mask:0xf\n\t"
        "v_add_f32_dpp v40, v40, v40 row_ror:4 row_mask:0xf bank_mask:0xf\n\t"
        "v_add_f32_dpp v41, v41, v41 row_ror:4 row_mask:0xf bank_mask:0xf\n\t"
        "v_add_f32_dpp v42, v42, v42 row_ror:4 row_mask:0xf bank_mask:0xf\n\t"
        "v_add_f32_dpp v43, v43, v43 row_ror:4 row_mask:0xf bank_mask:0xf\n\t"
        "v_add_f32_dpp v44, v44, v44 row_ror:4 row_mask:0xf bank_mask:0xf\n\t"
        "v_add_f32_dpp v20, v20, v20 row_ror:2 row_mask:0xf bank_mask:0xf\n\t"
        "v_add_f32_dpp v21, v21, v21 row_ror:2 row_mask:0xf bank_mask:0xf\n\t"
        "v_add_f32_dpp v22, v22, v22 row_ror:2 row_mask:0xf bank_mask:0xf\n\t"
        "v_add_f32_dpp v23, v23, v23 row_ror:2 row_mask:0xf bank_mask:0xf\n\t"
        "v_add_f32_dpp v24, v24, v24 row_ror:2 row_mask:0xf bank_mask:0xf\n\t"
        "v_add_f32_dpp v40, v40, v40 row_ror:2 row_mask:0xf bank_mask:0xf\n\t"
        "v_add_f32_dpp v41, v41, v41 row_ror:2 row_mask:0xf bank_mask:0xf\n\t"
        "v_add_f32_dpp v42, v42, v42 row_ror:2 row_mask:0xf bank_mask:0xf\n\t"
        "v_add_f32_dpp v43, v43, v43 row_ror:2 row_mask:0xf bank_mask:0xf\n\t"
        "v_add_f32_dpp v44, v44, v44 row_ror:2 row_mask:0xf bank_mask:0xf\n\t"
        "v_add_f32_dpp v20, v20, v20 row_ror:1 row_mask:0xf bank_mask:0xf\n\t"
        "v_add_f32_dpp v21, v21, v21 row_ror:1 row_mask:0xf bank_mask:0xf\n\t"
        "v_add_f32_dpp v22, v22, v22 row_ror:1 row_mask:0xf bank_mask:0xf\n\t"
        "v_add_f32_dpp v23, v23, v23 row_ror:1 row_mask:0xf bank_mask:0xf\n\t"
        "v_add_f32_dpp v24, v24, v24 row_ror:1 row_mask:0xf bank_mask:0xf\n\t"
        "v_add_f32_dpp v40, v40, v40 row_ror:1 row_mask:0xf bank_mask:0xf\n\t"
        "v_add_f32_dpp v41, v41, v41 row_ror:1 row_mask:0xf bank_mask:0xf\n\t"
        "v_add_f32_dpp v42, v42, v42 row_ror:1 row_mask:0xf bank_mask:0xf\n\t"
        "v_add_f32_dpp v43, v43, v43 row_ror:1 row_mask:0xf bank_mask:0xf\n\t"
        "v_add_f32_dpp v44, v44, v44 row_ror:1 row_mask:0xf bank_mask:0xf\n\t"
        "s_mov_b32 s52, 0x3b000000\n\t"
        "s_mov_b32 s53, 0x3727c5ac\n\t"
        "v_mul_f32 v20, s52, v20\n\t"
        "v_mul_f32 v40, s52, v40\n\t"
        "v_mul_f32 v21, s52, v21\n\t"
        "v_mul_f32 v41, s52, v41\n\t"
        "v_mul_f32 v22, s52, v22\n\t"
        "v_mul_f32 v42, s52, v42\n\t"
        "v_mul_f32 v23, s52, v23\n\t"
        "v_mul_f32 v43, s52, v43\n\t"
        "v_mul_f32 v24, s52, v24\n\t"
        "v_mul_f32 v44, s52, v44\n\t"
        "v_fma_f32 v40, -v20, v20, v40\n\t"
        "v_fma_f32 v41, -v21, v21, v41\n\t"
        "v_fma_f32 v42, -v22, v22, v42\n\t"
        "v_fma_f32 v43, -v23, v23, v43\n\t"
        "v_fma_f32 v44, -v24, v24, v44\n\t"
        "v_max_f32 v40, 0, v40\n\t"
        "v_max_f32 v41, 0, v41\n\t"
        "v_max_f32 v42, 0, v42\n\t"
        "v_max_f32 v43, 0, v43\n\t"
        "v_max_f32 v44, 0, v44\n\t"
        "v_add_f32 v40, s53, v40\n\t"
        "v_add_f32 v41, s53, v41\n\t"
        "v_add_f32 v42, s53, v42\n\t"
        "v_add_f32 v43, s53, v43\n\t"
        "v_add_f32 v44, s53, v44\n\t"
        "v_rsq_f32 v40, v40\n\t"
        "v_rsq_f32 v41, v41\n\t"
        "v_rsq_f32 v42, v42\n\t"
        "v_rsq_f32 v43, v43\n\t"
        "v_rsq_f32 v44, v44\n\t"
        "v_mul_f32 v20, -v20, v40\n\t"
        "v_mul_f32 v21, -v21, v41\n\t"
        "v_mul_f32 v22, -v22, v42\n\t"
        "v_mul_f32 v23, -v23, v43\n\t"
        "v_mul_f32 v24, -v24, v44\n\t"
        "s_waitcnt vmcnt(16)\n\t"
        "s_barrier\n\t"
        "ds_read_b64_tr_b16 a[0:1], v5 offset:0\n\t"
        "ds_read_b64_tr_b16 a[2:3], v5 offset:512\n\t"
        "ds_read_b64_tr_b16 a[4:5], v5 offset:4096\n\t"
        "ds_read_b64_tr_b16 a[6:7], v5 offset:4608\n\t"
        "ds_read_b64_tr_b16 a[8:9], v5 offset:8192\n\t"
        "ds_read_b64_tr_b16 a[10:11], v5 offset:8704\n\t"
        "ds_read_b64_tr_b16 a[12:13], v5 offset:12288\n\t"
        "ds_read_b64_tr_b16 a[14:15], v5 offset:12800\n\t"
        "ds_read_b64_tr_b16 a[16:17], v5 offset:16384\n\t"
        "ds_read_b64_tr_b16 a[18:19], v5 offset:16896\n\t"
        "ds_read_b64_tr_b16 a[20:21], v5 offset:20480\n\t"
        "ds_read_b64_tr_b16 a[22:23], v5 offset:20992\n\t"
        "ds_read_b64_tr_b16 a[24:25], v5 offset:24576\n\t"
        "ds_read_b64_tr_b16 a[26:27], v5 offset:25088\n\t"
        "ds_read_b64_tr_b16 a[28:29], v5 offset:28672\n\t"
        "ds_read_b64_tr_b16 a[30:31], v5 offset:29184\n\t"
        "ds_read_b64_tr_b16 a[32:33], v5 offset:32768\n\t"
        "ds_read_b64_tr_b16 a[34:35], v5 offset:33280\n\t"
        "ds_read_b64_tr_b16 a[36:37], v5 offset:36864\n\t"
        "ds_read_b64_tr_b16 a[38:39], v5 offset:37376\n\t"
        "ds_read_b64_tr_b16 a[40:41], v5 offset:40960\n\t"
        "ds_read_b64_tr_b16 a[42:43], v5 offset:41472\n\t"
        "ds_read_b64_tr_b16 a[44:45], v5 offset:45056\n\t"
        "ds_read_b64_tr_b16 a[46:47], v5 offset:45568\n\t"
        "ds_read_b64_tr_b16 a[48:49], v5 offset:49152\n\t"
        "ds_read_b64_tr_b16 a[50:51], v5 offset:49664\n\t"
        "ds_read_b64_tr_b16 a[52:53], v5 offset:53248\n\t"
        "ds_read_b64_tr_b16 a[54:55], v5 offset:53760\n\t"
        "ds_read_b64_tr_b16 a[56:57], v5 offset:57344\n\t"
        "ds_read_b64_tr_b16 a[58:59], v5 offset:57856\n\t"
        "ds_read_b64_tr_b16 a[60:61], v5 offset:61440\n\t"
        "ds_read_b64_tr_b16 a[62:63], v5 offset:61952\n\t"
        "s_waitcnt lgkmcnt(0)\n\t"
        "s_barrier\n\t"
        "s_sub_u32 s33, 3, s73\n\t"
        "v_lshrrev_b32 v6, s33, v1\n\t"
        "v_mul_lo_u32 v6, s74, v6\n\t"
        "s_lshr_b32 s36, 7, s73\n\t"
        "v_and_b32 v7, s36, v1\n\t"
        "v_lshl_add_u32 v6, v7, 4, v6\n\t"
        "s_lshl_b32 s36, s74, 3\n\t"
        "s_lshl_b32 s36, s36, s73\n\t"
        "s_lshl_b32 s37, s24, 12\n\t"
        "s_add_u32 m0, s37, 131072\n\t"
        "s_nop 0\n\t"
        "buffer_load_dwordx4 v6, s[60:63], s75 offen lds\n\t"
        "s_add_u32 m0, m0, 1024\n\t"
        "s_add_u32 s75, s75, s36\n\t"
        "buffer_load_dwordx4 v6, s[60:63], s75 offen lds\n\t"
        "s_add_u32 m0, m0, 1024\n\t"
        "s_add_u32 s75, s75, s36\n\t"
        "buffer_load_dwordx4 v6, s[60:63], s75 offen lds\n\t"
        "s_add_u32 m0, m0, 1024\n\t"
        "s_add_u32 s75, s75, s36\n\t"
        "buffer_load_dwordx4 v6, s[60:63], s75 offen lds\n\t"
        "s_mul_i32 s54, s25, 49152\n\t"
        "s_lshl_b32 s31, s24, 10\n\t"
        "s_add_u32 s54, s54, s31\n\t"
        "s_add_u32 s54, s54, 262144\n\t"
        "s_add_u32 s33, s54, 0\n\t"
        "buffer_load_dwordx4 v[60:63], v2, s[56:59], s33 offen sc1\n\t"
        "s_add_u32 s33, s54, 12288\n\t"
        "buffer_load_dwordx4 v[64:67], v2, s[56:59], s33 offen sc1\n\t"
        "s_add_u32 s33, s54, 24576\n\t"
        "buffer_load_dwordx4 v[68:71], v2, s[56:59], s33 offen sc1\n\t"
        "s_add_u32 s33, s54, 36864\n\t"
        "buffer_load_dwordx4 v[72:75], v2, s[56:59], s33 offen sc1\n\t"
        "v_readlane_b32 s4, v40, 0\n\t"
        "v_readlane_b32 s5, v20, 0\n\t"
        "v_readlane_b32 s6, v41, 0\n\t"
        "v_readlane_b32 s7, v21, 0\n\t"
        "v_readlane_b32 s8, v42, 0\n\t"
        "v_readlane_b32 s9, v22, 0\n\t"
        "v_readlane_b32 s10, v43, 0\n\t"
        "s_add_u32 s39, s28, 0\n\t"
        "s_and_b32 s39, s39, 3\n\t"
        "s_mul_i32 s37, s39, 98304\n\t"
        "s_lshl_b32 s38, s39, 12\n\t"
        "s_add_u32 s37, s37, s69\n\t"
        "s_add_u32 s38, s38, s70\n\t"
        "s_add_u32 m0, s38, 0\n\t"
        "s_nop 0\n\t"
        "buffer_load_dwordx4 v13, s[48:51], s37 offen lds\n\t"
        "v_readlane_b32 s11, v23, 0\n\t"
        "v_readlane_b32 s12, v44, 0\n\t"
        "v_readlane_b32 s13, v24, 0\n\t"
        "v_readlane_b32 s14, v40, 16\n\t"
        "v_readlane_b32 s15, v20, 16\n\t"
        "v_readlane_b32 s16, v41, 16\n\t"
        "v_readlane_b32 s17, v21, 16\n\t"
        "s_add_u32 m0, m0, 1024\n\t"
        "s_add_u32 s37, s37, 24576\n\t"
        "buffer_load_dwordx4 v18, s[48:51], s37 offen lds\n\t"
        "v_readlane_b32 s18, v42, 16\n\t"
        "v_readlane_b32 s19, v22, 16\n\t"
        "v_readlane_b32 s20, v43, 16\n\t"
        "v_readlane_b32 s21, v23, 16\n\t"
        "v_readlane_b32 s22, v44, 16\n\t"
        "v_readlane_b32 s23, v24, 16\n\t"
        "v_pk_fma_f32 v[96:97], v[96:97], s[4:5], s[4:5] op_sel:[0,0,1] op_sel_hi:[1,0,1]\n\t"
        "s_add_u32 m0, m0, 1024\n\t"
        "s_add_u32 s37, s37, 24576\n\t"
        "buffer_load_dwordx4 v13, s[48:51], s37 offen lds\n\t"
        "v_pk_fma_f32 v[98:99], v[98:99], s[4:5], s[4:5] op_sel:[0,0,1] op_sel_hi:[1,0,1]\n\t"
        "v_pk_fma_f32 v[100:101], v[100:101], s[4:5], s[4:5] op_sel:[0,0,1] op_sel_hi:[1,0,1]\n\t"
        "v_pk_fma_f32 v[102:103], v[102:103], s[4:5], s[4:5] op_sel:[0,0,1] op_sel_hi:[1,0,1]\n\t"
        "v_pk_fma_f32 v[104:105], v[104:105], s[6:7], s[6:7] op_sel:[0,0,1] op_sel_hi:[1,0,1]\n\t"
        "v_pk_fma_f32 v[106:107], v[106:107], s[6:7], s[6:7] op_sel:[0,0,1] op_sel_hi:[1,0,1]\n\t"
        "v_pk_fma_f32 v[108:109], v[108:109], s[6:7], s[6:7] op_sel:[0,0,1] op_sel_hi:[1,0,1]\n\t"
        "v_pk_fma_f32 v[110:111], v[110:111], s[6:7], s[6:7] op_sel:[0,0,1] op_sel_hi:[1,0,1]\n\t"
        "s_add_u32 m0, m0, 1024\n\t"
        "s_add_u32 s37, s37, 24576\n\t"
        "buffer_load_dwordx4 v18, s[48:51], s37 offen lds\n\t"
        "v_pk_fma_f32 v[112:113], v[112:113], s[8:9], s[8:9] op_sel:[0,0,1] op_sel_hi:[1,0,1]\n\t"
        "v_pk_fma_f32 v[114:115], v[114:115], s[8:9], s[8:9] op_sel:[0,0,1] op_sel_hi:[1,0,1]\n\t"
        "v_pk_fma_f32 v[116:117], v[116:117], s[8:9], s[8:9] op_sel:[0,0,1] op_sel_hi:[1,0,1]\n\t"
        "v_pk_fma_f32 v[118:119], v[118:119], s[8:9], s[8:9] op_sel:[0,0,1] op_sel_hi:[1,0,1]\n\t"
        "v_pk_fma_f32 v[120:121], v[120:121], s[10:11], s[10:11] op_sel:[0,0,1] op_sel_hi:[1,0,1]\n\t"
        "v_pk_fma_f32 v[122:123], v[122:123], s[10:11], s[10:11] op_sel:[0,0,1] op_sel_hi:[1,0,1]\n\t"
        "v_pk_fma_f32 v[124:125], v[124:125], s[10:11], s[10:11] op_sel:[0,0,1] op_sel_hi:[1,0,1]\n\t"
        "s_add_u32 s39, s28, 1\n\t"
        "s_and_b32 s39, s39, 3\n\t"
        "s_mul_i32 s37, s39, 98304\n\t"
        "s_lshl_b32 s38, s39, 12\n\t"
        "s_add_u32 s37, s37, s69\n\t"
        "s_add_u32 s38, s38, s70\n\t"
        "s_add_u32 m0, s38, 0\n\t"
        "s_nop 0\n\t"
        "buffer_load_dwordx4 v13, s[48:51], s37 offen lds\n\t"
        "v_pk_fma_f32 v[126:127], v[126:127], s[10:11], s[10:11] op_sel:[0,0,1] op_sel_hi:[1,0,1]\n\t"
        "v_pk_fma_f32 v[128:129], v[128:129], s[12:13], s[12:13] op_sel:[0,0,1] op_sel_hi:[1,0,1]\n\t"
        "v_pk_fma_f32 v[130:131], v[130:131], s[12:13], s[12:13] op_sel:[0,0,1] op_sel_hi:[1,0,1]\n\t"
        "v_pk_fma_f32 v[132:133], v[132:133], s[12:13], s[12:13] op_sel:[0,0,1] op_sel_hi:[1,0,1]\n\t"
        "v_pk_fma_f32 v[134:135], v[134:135], s[12:13], s[12:13] op_sel:[0,0,1] op_sel_hi:[1,0,1]\n\t"
        "v_pk_fma_f32 v[136:137], v[136:137], s[14:15], s[14:15] op_sel:[0,0,1] op_sel_hi:[1,0,1]\n\t"
        "v_pk_fma_f32 v[138:139], v[138:139], s[14:15], s[14:15] op_sel:[0,0,1] op_sel_hi:[1,0,1]\n\t"
        "s_add_u32 m0, m0, 1024\n\t"
        "s_add_u32 s37, s37, 24576\n\t"
        "buffer_load_dwordx4 v18, s[48:51], s37 offen lds\n\t"
        "v_pk_fma_f32 v[140:141], v[140:141], s[14:15], s[14:15] op_sel:[0,0,1] op_sel_hi:[1,0,1]\n\t"
        "v_pk_fma_f32 v[142:143], v[142:143], s[14:15], s[14:15] op_sel:[0,0,1] op_sel_hi:[1,0,1]\n\t"
        "v_pk_fma_f32 v[144:145], v[144:145], s[16:17], s[16:17] op_sel:[0,0,1] op_sel_hi:[1,0,1]\n\t"
        "v_pk_fma_f32 v[146:147], v[146:147], s[16:17], s[16:17] op_sel:[0,0,1] op_sel_hi:[1,0,1]\n\t"
        "v_pk_fma_f32 v[148:149], v[148:149], s[16:17], s[16:17] op_sel:[0,0,1] op_sel_hi:[1,0,1]\n\t"
        "v_pk_fma_f32 v[150:151], v[150:151], s[16:17], s[16:17] op_sel:[0,0,1] op_sel_hi:[1,0,1]\n\t"
        "v_pk_fma_f32 v[152:153], v[152:153], s[18:19], s[18:19] op_sel:[0,0,1] op_sel_hi:[1,0,1]\n\t"
        "s_add_u32 m0, m0, 1024\n\t"
        "s_add_u32 s37, s37, 24576\n\t"
        "buffer_load_dwordx4 v13, s[48:51], s37 offen lds\n\t"
        "v_pk_fma_f32 v[154:155], v[154:155], s[18:19], s[18:19] op_sel:[0,0,1] op_sel_hi:[1,0,1]\n\t"
        "v_pk_fma_f32 v[156:157], v[156:157], s[18:19], s[18:19] op_sel:[0,0,1] op_sel_hi:[1,0,1]\n\t"
        "v_pk_fma_f32 v[158:159], v[158:159], s[18:19], s[18:19] op_sel:[0,0,1] op_sel_hi:[1,0,1]\n\t"
        "v_pk_fma_f32 v[160:161], v[160:161], s[20:21], s[20:21] op_sel:[0,0,1] op_sel_hi:[1,0,1]\n\t"
        "v_pk_fma_f32 v[162:163], v[162:163], s[20:21], s[20:21] op_sel:[0,0,1] op_sel_hi:[1,0,1]\n\t"
        "v_pk_fma_f32 v[164:165], v[164:165], s[20:21], s[20:21] op_sel:[0,0,1] op_sel_hi:[1,0,1]\n\t"
        "v_pk_fma_f32 v[166:167], v[166:167], s[20:21], s[20:21] op_sel:[0,0,1] op_sel_hi:[1,0,1]\n\t"
        "s_add_u32 m0, m0, 1024\n\t"
        "s_add_u32 s37, s37, 24576\n\t"
        "buffer_load_dwordx4 v18, s[48:51], s37 offen lds\n\t"
        "v_pk_fma_f32 v[168:169], v[168:169], s[22:23], s[22:23] op_sel:[0,0,1] op_sel_hi:[1,0,1]\n\t"
        "v_pk_fma_f32 v[170:171], v[170:171], s[22:23], s[22:23] op_sel:[0,0,1] op_sel_hi:[1,0,1]\n\t"
        "v_pk_fma_f32 v[172:173], v[172:173], s[22:23], s[22:23] op_sel:[0,0,1] op_sel_hi:[1,0,1]\n\t"
        "v_pk_fma_f32 v[174:175], v[174:175], s[22:23], s[22:23] op_sel:[0,0,1] op_sel_hi:[1,0,1]\n\t"
        "v_readlane_b32 s4, v40, 32\n\t"
        "v_readlane_b32 s5, v20, 32\n\t"
        "v_readlane_b32 s6, v41, 32\n\t"
        "s_add_u32 s39, s28, 2\n\t"
        "s_and_b32 s39, s39, 3\n\t"
        "s_mul_i32 s37, s39, 98304\n\t"
        "s_lshl_b32 s38, s39, 12\n\t"
        "s_add_u32 s37, s37, s69\n\t"
        "s_add_u32 s38, s38, s70\n\t"
        "s_add_u32 m0, s38, 0\n\t"
        "s_nop 0\n\t"
        "buffer_load_dwordx4 v13, s[48:51], s37 offen lds\n\t"
        "v_readlane_b32 s7, v21, 32\n\t"
        "v_readlane_b32 s8, v42, 32\n\t"
        "v_readlane_b32 s9, v22, 32\n\t"
        "v_readlane_b32 s10, v43, 32\n\t"
        "v_readlane_b32 s11, v23, 32\n\t"
        "v_readlane_b32 s12, v44, 32\n\t"
        "v_readlane_b32 s13, v24, 32\n\t"
        "s_add_u32 m0, m0, 1024\n\t"
        "s_add_u32 s37, s37, 24576\n\t"
        "buffer_load_dwordx4 v18, s[48:51], s37 offen lds\n\t"
        "v_readlane_b32 s14, v40, 48\n\t"
        "v_readlane_b32 s15, v20, 48\n\t"
        "v_readlane_b32 s16, v41, 48\n\t"
        "v_readlane_b32 s17, v21, 48\n\t"
        "v_readlane_b32 s18, v42, 48\n\t"
        "v_readlane_b32 s19, v22, 48\n\t"
        "v_readlane_b32 s20, v43, 48\n\t"
        "s_add_u32 m0, m0, 1024\n\t"
        "s_add_u32 s37, s37, 24576\n\t"
        "buffer_load_dwordx4 v13, s[48:51], s37 offen lds\n\t"
        "v_readlane_b32 s21, v23, 48\n\t"
        "v_readlane_b32 s22, v44, 48\n\t"
        "v_readlane_b32 s23, v24, 48\n\t"
        "v_pk_fma_f32 v[176:177], v[176:177], s[4:5], s[4:5] op_sel:[0,0,1] op_sel_hi:[1,0,1]\n\t"
        "v_pk_fma_f32 v[178:179], v[178:179], s[4:5], s[4:5] op_sel:[0,0,1] op_sel_hi:[1,0,1]\n\t"
        "v_pk_fma_f32 v[180:181], v[180:181], s[4:5], s[4:5] op_sel:[0,0,1] op_sel_hi:[1,0,1]\n\t"
        "v_pk_fma_f32 v[182:183], v[182:183], s[4:5], s[4:5] op_sel:[0,0,1] op_sel_hi:[1,0,1]\n\t"
        "s_add_u32 m0, m0, 1024\n\t"
        "s_add_u32 s37, s37, 24576\n\t"
        "buffer_load_dwordx4 v18, s[48:51], s37 offen lds\n\t"
        "v_pk_fma_f32 v[184:185], v[184:185], s[6:7], s[6:7] op_sel:[0,0,1] op_sel_hi:[1,0,1]\n\t"
        "v_pk_fma_f32 v[186:187], v[186:187], s[6:7], s[6:7] op_sel:[0,0,1] op_sel_hi:[1,0,1]\n\t"
        "v_pk_fma_f32 v[188:189], v[188:189], s[6:7], s[6:7] op_sel:[0,0,1] op_sel_hi:[1,0,1]\n\t"
        "v_pk_fma_f32 v[190:191], v[190:191], s[6:7], s[6:7] op_sel:[0,0,1] op_sel_hi:[1,0,1]\n\t"
        "v_pk_fma_f32 v[192:193], v[192:193], s[8:9], s[8:9] op_sel:[0,0,1] op_sel_hi:[1,0,1]\n\t"
        "v_pk_fma_f32 v[194:195], v[194:195], s[8:9], s[8:9] op_sel:[0,0,1] op_sel_hi:[1,0,1]\n\t"
        "v_pk_fma_f32 v[196:197], v[196:197], s[8:9], s[8:9] op_sel:[0,0,1] op_sel_hi:[1,0,1]\n\t"
        "s_add_u32 s39, s28, 3\n\t"
        "s_and_b32 s39, s39, 3\n\t"
        "s_mul_i32 s37, s39, 98304\n\t"
        "s_lshl_b32 s38, s39, 12\n\t"
        "s_add_u32 s37, s37, s69\n\t"
        "s_add_u32 s38, s38, s70\n\t"
        "s_add_u32 m0, s38, 0\n\t"
        "s_nop 0\n\t"
        "buffer_load_dwordx4 v13, s[48:51], s37 offen lds\n\t"
        "v_pk_fma_f32 v[198:199], v[198:199], s[8:9], s[8:9] op_sel:[0,0,1] op_sel_hi:[1,0,1]\n\t"
        "v_pk_fma_f32 v[200:201], v[200:201], s[10:11], s[10:11] op_sel:[0,0,1] op_sel_hi:[1,0,1]\n\t"
        "v_pk_fma_f32 v[202:203], v[202:203], s[10:11], s[10:11] op_sel:[0,0,1] op_sel_hi:[1,0,1]\n\t"
        "v_pk_fma_f32 v[204:205], v[204:205], s[10:11], s[10:11] op_sel:[0,0,1] op_sel_hi:[1,0,1]\n\t"
        "v_pk_fma_f32 v[206:207], v[206:207], s[10:11], s[10:11] op_sel:[0,0,1] op_sel_hi:[1,0,1]\n\t"
        "v_pk_fma_f32 v[208:209], v[208:209], s[12:13], s[12:13] op_sel:[0,0,1] op_sel_hi:[1,0,1]\n\t"
        "v_pk_fma_f32 v[210:211], v[210:211], s[12:13], s[12:13] op_sel:[0,0,1] op_sel_hi:[1,0,1]\n\t"
        "s_add_u32 m0, m0, 1024\n\t"
        "s_add_u32 s37, s37, 24576\n\t"
        "buffer_load_dwordx4 v18, s[48:51], s37 offen lds\n\t"
        "v_pk_fma_f32 v[212:213], v[212:213], s[12:13], s[12:13] op_sel:[0,0,1] op_sel_hi:[1,0,1]\n\t"
        "v_pk_fma_f32 v[214:215], v[214:215], s[12:13], s[12:13] op_sel:[0,0,1] op_sel_hi:[1,0,1]\n\t"
        "v_pk_fma_f32 v[216:217], v[216:217], s[14:15], s[14:15] op_sel:[0,0,1] op_sel_hi:[1,0,1]\n\t"
        "v_pk_fma_f32 v[218:219], v[218:219], s[14:15], s[14:15] op_sel:[0,0,1] op_sel_hi:[1,0,1]\n\t"
        "v_pk_fma_f32 v[220:221], v[220:221], s[14:15], s[14:15] op_sel:[0,0,1] op_sel_hi:[1,0,1]\n\t"
        "v_pk_fma_f32 v[222:223], v[222:223], s[14:15], s[14:15] op_sel:[0,0,1] op_sel_hi:[1,0,1]\n\t"
        "v_pk_fma_f32 v[224:225], v[224:225], s[16:17], s[16:17] op_sel:[0,0,1] op_sel_hi:[1,0,1]\n\t"
        "s_add_u32 m0, m0, 1024\n\t"
        "s_add_u32 s37, s37, 24576\n\t"
        "buffer_load_dwordx4 v13, s[48:51], s37 offen lds\n\t"
        "v_pk_fma_f32 v[226:227], v[226:227], s[16:17], s[16:17] op_sel:[0,0,1] op_sel_hi:[1,0,1]\n\t"
        "v_pk_fma_f32 v[228:229], v[228:229], s[16:17], s[16:17] op_sel:[0,0,1] op_sel_hi:[1,0,1]\n\t"
        "v_pk_fma_f32 v[230:231], v[230:231], s[16:17], s[16:17] op_sel:[0,0,1] op_sel_hi:[1,0,1]\n\t"
        "v_pk_fma_f32 v[232:233], v[232:233], s[18:19], s[18:19] op_sel:[0,0,1] op_sel_hi:[1,0,1]\n\t"
        "v_pk_fma_f32 v[234:235], v[234:235], s[18:19], s[18:19] op_sel:[0,0,1] op_sel_hi:[1,0,1]\n\t"
        "v_pk_fma_f32 v[236:237], v[236:237], s[18:19], s[18:19] op_sel:[0,0,1] op_sel_hi:[1,0,1]\n\t"
        "v_pk_fma_f32 v[238:239], v[238:239], s[18:19], s[18:19] op_sel:[0,0,1] op_sel_hi:[1,0,1]\n\t"
        "s_add_u32 m0, m0, 1024\n\t"
        "s_add_u32 s37, s37, 24576\n\t"
        "buffer_load_dwordx4 v18, s[48:51], s37 offen lds\n\t"
        "v_pk_fma_f32 v[240:241], v[240:241], s[20:21], s[20:21] op_sel:[0,0,1] op_sel_hi:[1,0,1]\n\t"
        "v_pk_fma_f32 v[242:243], v[242:243], s[20:21], s[20:21] op_sel:[0,0,1] op_sel_hi:[1,0,1]\n\t"
        "v_pk_fma_f32 v[244:245], v[244:245], s[20:21], s[20:21] op_sel:[0,0,1] op_sel_hi:[1,0,1]\n\t"
        "v_pk_fma_f32 v[246:247], v[246:247], s[20:21], s[20:21] op_sel:[0,0,1] op_sel_hi:[1,0,1]\n\t"
        "v_pk_fma_f32 v[248:249], v[248:249], s[22:23], s[22:23] op_sel:[0,0,1] op_sel_hi:[1,0,1]\n\t"
        "v_pk_fma_f32 v[250:251], v[250:251], s[22:23], s[22:23] op_sel:[0,0,1] op_sel_hi:[1,0,1]\n\t"
        "v_pk_fma_f32 v[252:253], v[252:253], s[22:23], s[22:23] op_sel:[0,0,1] op_sel_hi:[1,0,1]\n\t"
        "v_pk_fma_f32 v[254:255], v[254:255], s[22:23], s[22:23] op_sel:[0,0,1] op_sel_hi:[1,0,1]\n\t"
        "v_lshrrev_b32 v6, 2, v1\n\t"
        "v_and_b32 v6, 3, v6\n\t"
        "v_lshl_add_u32 v6, v3, 3, v6\n\t"
        "s_sub_u32 s31, 7, s73\n\t"
        "v_lshlrev_b32 v6, s31, v6\n\t"
        "v_and_b32 v7, 3, v1\n\t"
        "v_lshl_add_u32 v11, v7, 3, v6\n\t"
        "s_lshl_b32 s31, s24, 12\n\t"
        "s_add_u32 s31, s31, 131072\n\t"
        "v_add_u32 v11, s31, v11\n\t"
        "s_cmp_eq_u32 s73, 0\n\t"
        "s_mov_b32 s31, 2048\n\t"
        "s_cselect_b32 s31, 64, s31\n\t"
        "s_mov_b32 s33, 256\n\t"
        "s_cselect_b32 s33, 512, s33\n\t"
        "s_mov_b32 s36, 16384\n\t"
        "s_cselect_b32 s36, 1024, s36\n\t"
        "s_mov_b32 s37, 1024\n\t"
        "s_cselect_b32 s37, 2048, s37\n\t"
        "s_waitcnt vmcnt(20)\n\t"
        "v_add_u32 v6, 0, v11\n\t"
        "v_add_u32 v7, s33, v6\n\t"
        "ds_read_b64_tr_b16 v[20:21], v6\n\t"
        "ds_read_b64_tr_b16 v[22:23], v7\n\t"
        "v_add_u32 v6, 32, v11\n\t"
        "v_add_u32 v7, s33, v6\n\t"
        "ds_read_b64_tr_b16 v[24:25], v6\n\t"
        "ds_read_b64_tr_b16 v[26:27], v7\n\t"
        "v_add_u32 v6, 0, v11\n\t"
        "v_add_u32 v6, s31, v6\n\t"
        "v_add_u32 v7, s33, v6\n\t"
        "ds_read_b64_tr_b16 v[28:29], v6\n\t"
        "ds_read_b64_tr_b16 v[30:31], v7\n\t"
        "v_add_u32 v6, 32, v11\n\t"
        "v_add_u32 v6, s31, v6\n\t"
        "v_add_u32 v7, s33, v6\n\t"
        "ds_read_b64_tr_b16 v[32:33], v6\n\t"
        "ds_read_b64_tr_b16 v[34:35], v7\n\t"
        "s_mov_b32 s38, s72\n\t"
        "s_waitcnt lgkmcnt(6)\n\t"
        "buffer_store_dwordx4 v[20:23], v2, s[56:59], s38 offen sc1\n\t"
        "s_mov_b32 s38, s72\n\t"
        "s_add_u32 s38, s38, s36\n\t"
        "s_waitcnt lgkmcnt(4)\n\t"
        "buffer_store_dwordx4 v[24:27], v2, s[56:59], s38 offen sc1\n\t"
        "s_mov_b32 s38, s72\n\t"
        "s_add_u32 s38, s38, s37\n\t"
        "s_waitcnt lgkmcnt(2)\n\t"
        "buffer_store_dwordx4 v[28:31], v2, s[56:59], s38 offen sc1\n\t"
        "s_mov_b32 s38, s72\n\t"
        "s_add_u32 s38, s38, s36\n\t"
        "s_add_u32 s38, s38, s37\n\t"
        "s_waitcnt lgkmcnt(0)\n\t"
        "buffer_store_dwordx4 v[32:35], v2, s[56:59], s38 offen sc1\n\t"
        "s_barrier\n\t"
        "ds_read_b64_tr_b16 a[64:65], v10 offset:0\n\t"
        "ds_read_b64_tr_b16 a[66:67], v10 offset:512\n\t"
        "ds_read_b64_tr_b16 a[68:69], v10 offset:4096\n\t"
        "ds_read_b64_tr_b16 a[70:71], v10 offset:4608\n\t"
        "ds_read_b64_tr_b16 a[72:73], v10 offset:8192\n\t"
        "ds_read_b64_tr_b16 a[74:75], v10 offset:8704\n\t"
        "ds_read_b64_tr_b16 a[76:77], v10 offset:12288\n\t"
        "ds_read_b64_tr_b16 a[78:79], v10 offset:12800\n\t"
        "ds_read_b64_tr_b16 a[80:81], v10 offset:16384\n\t"
        "ds_read_b64_tr_b16 a[82:83], v10 offset:16896\n\t"
        "ds_read_b64_tr_b16 a[84:85], v10 offset:20480\n\t"
        "ds_read_b64_tr_b16 a[86:87], v10 offset:20992\n\t"
        "ds_read_b64_tr_b16 a[88:89], v10 offset:24576\n\t"
        "ds_read_b64_tr_b16 a[90:91], v10 offset:25088\n\t"
        "ds_read_b64_tr_b16 a[92:93], v10 offset:28672\n\t"
        "ds_read_b64_tr_b16 a[94:95], v10 offset:29184\n\t"
        "ds_read_b64_tr_b16 a[96:97], v10 offset:32768\n\t"
        "ds_read_b64_tr_b16 a[98:99], v10 offset:33280\n\t"
        "ds_read_b64_tr_b16 a[100:101], v10 offset:36864\n\t"
        "ds_read_b64_tr_b16 a[102:103], v10 offset:37376\n\t"
        "ds_read_b64_tr_b16 a[104:105], v10 offset:40960\n\t"
        "ds_read_b64_tr_b16 a[106:107], v10 offset:41472\n\t"
        "ds_read_b64_tr_b16 a[108:109], v10 offset:45056\n\t"
        "ds_read_b64_tr_b16 a[110:111], v10 offset:45568\n\t"
        "ds_read_b64_tr_b16 a[112:113], v10 offset:49152\n\t"
        "ds_read_b64_tr_b16 a[114:115], v10 offset:49664\n\t"
        "ds_read_b64_tr_b16 a[116:117], v10 offset:53248\n\t"
        "ds_read_b64_tr_b16 a[118:119], v10 offset:53760\n\t"
        "ds_read_b64_tr_b16 a[120:121], v10 offset:57344\n\t"
        "ds_read_b64_tr_b16 a[122:123], v10 offset:57856\n\t"
        "ds_read_b64_tr_b16 a[124:125], v10 offset:61440\n\t"
        "ds_read_b64_tr_b16 a[126:127], v10 offset:61952\n\t"
        "s_waitcnt vmcnt(23)\n\t"
        "v_xor_b32 v6, s34, v61\n\t"
        "v_bitop3_b32 v6, v63, s34, v6 bitop3:0xbe\n\t"
        "s_waitcnt vmcnt(22)\n\t"
        "v_bitop3_b32 v6, v65, s34, v6 bitop3:0xbe\n\t"
        "v_bitop3_b32 v6, v67, s34, v6 bitop3:0xbe\n\t"
        "s_waitcnt vmcnt(21)\n\t"
        "v_bitop3_b32 v6, v69, s34, v6 bitop3:0xbe\n\t"
        "v_bitop3_b32 v6, v71, s34, v6 bitop3:0xbe\n\t"
        "s_waitcnt vmcnt(20)\n\t"
        "v_bitop3_b32 v6, v73, s34, v6 bitop3:0xbe\n\t"
        "v_bitop3_b32 v6, v75, s34, v6 bitop3:0xbe\n\t"
        "v_cmp_ne_u32 vcc, 0, v6\n\t"
        "s_nop 0\n\t"
        "s_cbranch_vccz .Lg1ok5\n\t"
        "s_mov_b32 s31, 65536\n\t"
        ".Lg1sp6:\n\t"
        "s_add_u32 s33, s54, 0\n\t"
        "buffer_load_dwordx4 v[60:63], v2, s[56:59], s33 offen sc1\n\t"
        "s_add_u32 s33, s54, 12288\n\t"
        "buffer_load_dwordx4 v[64:67], v2, s[56:59], s33 offen sc1\n\t"
        "s_add_u32 s33, s54, 24576\n\t"
        "buffer_load_dwordx4 v[68:71], v2, s[56:59], s33 offen sc1\n\t"
        "s_add_u32 s33, s54, 36864\n\t"
        "buffer_load_dwordx4 v[72:75], v2, s[56:59], s33 offen sc1\n\t"
        "s_waitcnt vmcnt(0)\n\t"
        "v_xor_b32 v6, s34, v61\n\t"
        "v_bitop3_b32 v6, v63, s34, v6 bitop3:0xbe\n\t"
        "v_bitop3_b32 v6, v65, s34, v6 bitop3:0xbe\n\t"
        "v_bitop3_b32 v6, v67, s34, v6 bitop3:0xbe\n\t"
        "v_bitop3_b32 v6, v69, s34, v6 bitop3:0xbe\n\t"
        "v_bitop3_b32 v6, v71, s34, v6 bitop3:0xbe\n\t"
        "v_bitop3_b32 v6, v73, s34, v6 bitop3:0xbe\n\t"
        "v_bitop3_b32 v6, v75, s34, v6 bitop3:0xbe\n\t"
        "v_cmp_ne_u32 vcc, 0, v6\n\t"
        "s_nop 0\n\t"
        "s_cbranch_vccz .Lg1ok5\n\t"
        "s_sub_u32 s31, s31, 1\n\t"
        "s_cmp_lg_u32 s31, 0\n\t"
        "s_cbranch_scc1 .Lg1sp6\n\t"
        ".Lg1ok5:\n\t"
        "v_lshlrev_b32 v16, 16, v60\n\t"
        "v_and_b32 v17, s42, v60\n\t"
        "v_lshlrev_b32 v18, 16, v62\n\t"
        "v_and_b32 v19, s42, v62\n\t"
        "v_lshlrev_b32 v6, 16, v64\n\t"
        "v_and_b32 v7, s42, v64\n\t"
        "v_pk_add_f32 v[16:17], v[16:17], v[6:7]\n\t"
        "v_lshlrev_b32 v6, 16, v66\n\t"
        "v_and_b32 v7, s42, v66\n\t"
        "v_pk_add_f32 v[18:19], v[18:19], v[6:7]\n\t"
        "v_lshlrev_b32 v6, 16, v68\n\t"
        "v_and_b32 v7, s42, v68\n\t"
        "v_pk_add_f32 v[16:17], v[16:17], v[6:7]\n\t"
        "v_lshlrev_b32 v6, 16, v70\n\t"
        "v_and_b32 v7, s42, v70\n\t"
        "v_pk_add_f32 v[18:19], v[18:19], v[6:7]\n\t"
        "v_lshlrev_b32 v6, 16, v72\n\t"
        "v_and_b32 v7, s42, v72\n\t"
        "v_pk_add_f32 v[16:17], v[16:17], v[6:7]\n\t"
        "v_lshlrev_b32 v6, 16, v74\n\t"
        "v_and_b32 v7, s42, v74\n\t"
        "v_pk_add_f32 v[18:19], v[18:19], v[6:7]\n\t"
        "v_cvt_pk_bf16_f32 v60, v16, v17\n\t"
        "v_cvt_pk_bf16_f32 v61, v18, v19\n\t"
        "s_cmp_lt_u32 s24, 2\n\t"
        "s_cbranch_scc1 .Lg1sa7\n\t"
        "v_lshlrev_b32 v6, 16, v60\n\t"
        "v_and_b32 v7, s42, v60\n\t"
        "v_add_f32 v6, 1.0, v6\n\t"
        "v_add_f32 v7, 1.0, v7\n\t"
        "v_cvt_pk_bf16_f32 v60, v6, v7\n\t"
        "v_lshlrev_b32 v6, 16, v61\n\t"
        "v_and_b32 v7, s42, v61\n\t"
        "v_add_f32 v6, 1.0, v6\n\t"
        "v_add_f32 v7, 1.0, v7\n\t"
        "v_cvt_pk_bf16_f32 v61, v6, v7\n\t"
        ".Lg1sa7:\n\t"
        "v_lshlrev_b32 v16, 16, v60\n\t"
        "v_and_b32 v17, s42, v60\n\t"
        "v_lshlrev_b32 v18, 16, v61\n\t"
        "v_and_b32 v19, s42, v61\n\t"
        "s_lshl_b32 s31, s24, 10\n\t"
        "s_add_u32 s31, s31, 147456\n\t"
        "v_add_u32 v6, s31, v2\n\t"
        "ds_write_b128 v6, v[16:19]\n\t"
        "s_waitcnt lgkmcnt(0)\n\t"
        "s_barrier\n\t"
        "v_lshlrev_b32 v7, 1, v2\n\t"
        "v_add_u32 v7, 147456, v7\n\t"
        "ds_read_b128 v[16:19], v7\n\t"
        "ds_read_b128 v[20:23], v7 offset:16\n\t"
        "ds_read_b128 v[24:27], v7 offset:2048\n\t"
        "ds_read_b128 v[28:31], v7 offset:2064\n\t"
        "v_lshrrev_b32 v6, 2, v1\n\t"
        "v_and_b32 v7, 3, v1\n\t"
        "v_and_b32 v8, 1, v6\n\t"
        "v_lshl_add_u32 v8, v8, 2, v7\n\t"
        "v_lshlrev_b32 v6, 10, v6\n\t"
        "v_lshl_add_u32 v6, v7, 8, v6\n\t"
        "v_lshl_add_u32 v6, v8, 4, v6\n\t"
        "s_lshl_b32 s31, s24, 6\n\t"
        "v_xor_b32 v6, s31, v6\n\t"
        "v_xor_b32 v7, 0, v6\n\t"
        "v_add_u32 v10, 65536, v7\n\t"
        "v_add_u32 v32, 131072, v7\n\t"
        "v_xor_b32 v7, 16, v6\n\t"
        "v_add_u32 v11, 65536, v7\n\t"
        "v_add_u32 v33, 131072, v7\n\t"
        "v_xor_b32 v7, 32, v6\n\t"
        "v_add_u32 v12, 65536, v7\n\t"
        "v_add_u32 v34, 131072, v7\n\t"
        "v_xor_b32 v7, 48, v6\n\t"
        "v_add_u32 v13, 65536, v7\n\t"
        "v_add_u32 v35, 131072, v7\n\t"
        "v_xor_b32 v6, v4, v3\n\t"
        "v_lshlrev_b32 v6, 4, v6\n\t"
        "v_lshl_add_u32 v6, v3, 8, v6\n\t"
        "v_xor_b32 v7, 64, v6\n\t"
        "v_add_u32 v36, 65536, v6\n\t"
        "v_add_u32 v38, 131072, v6\n\t"
        "v_add_u32 v37, 65536, v7\n\t"
        "v_add_u32 v39, 131072, v7\n\t"
        "s_waitcnt lgkmcnt(1)\n\t"
        "v_pk_fma_f32 v[96:97], v[96:97], v[24:25], v[16:17]\n\t"
        "v_pk_fma_f32 v[98:99], v[98:99], v[26:27], v[18:19]\n\t"
        "s_waitcnt lgkmcnt(0)\n\t"
        "v_pk_fma_f32 v[100:101], v[100:101], v[28:29], v[20:21]\n\t"
        "v_pk_fma_f32 v[102:103], v[102:103], v[30:31], v[22:23]\n\t"
        "v_cvt_pk_bf16_f32 v40, v96, v97\n\t"
        "v_cvt_pk_bf16_f32 v41, v98, v99\n\t"
        "v_cvt_pk_bf16_f32 v42, v100, v101\n\t"
        "v_cvt_pk_bf16_f32 v43, v102, v103\n\t"
        "ds_write_b128 v32, v[40:43]\n\t"
        "v_pk_fma_f32 v[104:105], v[104:105], v[24:25], v[16:17]\n\t"
        "v_pk_fma_f32 v[106:107], v[106:107], v[26:27], v[18:19]\n\t"
        "v_pk_fma_f32 v[108:109], v[108:109], v[28:29], v[20:21]\n\t"
        "v_pk_fma_f32 v[110:111], v[110:111], v[30:31], v[22:23]\n\t"
        "v_cvt_pk_bf16_f32 v44, v104, v105\n\t"
        "v_cvt_pk_bf16_f32 v45, v106, v107\n\t"
        "v_cvt_pk_bf16_f32 v46, v108, v109\n\t"
        "v_cvt_pk_bf16_f32 v47, v110, v111\n\t"
        "ds_write_b128 v33, v[44:47]\n\t"
        "v_pk_fma_f32 v[112:113], v[112:113], v[24:25], v[16:17]\n\t"
        "v_pk_fma_f32 v[114:115], v[114:115], v[26:27], v[18:19]\n\t"
        "v_pk_fma_f32 v[116:117], v[116:117], v[28:29], v[20:21]\n\t"
        "v_pk_fma_f32 v[118:119], v[118:119], v[30:31], v[22:23]\n\t"
        "v_cvt_pk_bf16_f32 v40, v112, v113\n\t"
        "v_cvt_pk_bf16_f32 v41, v114, v115\n\t"
        "v_cvt_pk_bf16_f32 v42, v116, v117\n\t"
        "v_cvt_pk_bf16_f32 v43, v118, v119\n\t"
        "ds_write_b128 v34, v[40:43]\n\t"
        "v_pk_fma_f32 v[120:121], v[120:121], v[24:25], v[16:17]\n\t"
        "v_pk_fma_f32 v[122:123], v[122:123], v[26:27], v[18:19]\n\t"
        "v_pk_fma_f32 v[124:125], v[124:125], v[28:29], v[20:21]\n\t"
        "v_pk_fma_f32 v[126:127], v[126:127], v[30:31], v[22:23]\n\t"
        "v_cvt_pk_bf16_f32 v44, v120, v121\n\t"
        "v_cvt_pk_bf16_f32 v45, v122, v123\n\t"
        "v_cvt_pk_bf16_f32 v46, v124, v125\n\t"
        "v_cvt_pk_bf16_f32 v47, v126, v127\n\t"
        "ds_write_b128 v35, v[44:47]\n\t"
        "v_pk_fma_f32 v[128:129], v[128:129], v[24:25], v[16:17]\n\t"
        "v_pk_fma_f32 v[130:131], v[130:131], v[26:27], v[18:19]\n\t"
        "v_pk_fma_f32 v[132:133], v[132:133], v[28:29], v[20:21]\n\t"
        "v_pk_fma_f32 v[134:135], v[134:135], v[30:31], v[22:23]\n\t"
        "v_cvt_pk_bf16_f32 v40, v128, v129\n\t"
        "v_cvt_pk_bf16_f32 v41, v130, v131\n\t"
        "v_cvt_pk_bf16_f32 v42, v132, v133\n\t"
        "v_cvt_pk_bf16_f32 v43, v134, v135\n\t"
        "ds_write_b128 v10, v[40:43] offset:0\n\t"
        "v_pk_fma_f32 v[136:137], v[136:137], v[24:25], v[16:17]\n\t"
        "v_pk_fma_f32 v[138:139], v[138:139], v[26:27], v[18:19]\n\t"
        "v_pk_fma_f32 v[140:141], v[140:141], v[28:29], v[20:21]\n\t"
        "v_pk_fma_f32 v[142:143], v[142:143], v[30:31], v[22:23]\n\t"
        "v_cvt_pk_bf16_f32 v44, v136, v137\n\t"
        "v_cvt_pk_bf16_f32 v45, v138, v139\n\t"
        "v_cvt_pk_bf16_f32 v46, v140, v141\n\t"
        "v_cvt_pk_bf16_f32 v47, v142, v143\n\t"
        "ds_write_b128 v11, v[44:47] offset:0\n\t"
        "v_pk_fma_f32 v[144:145], v[144:145], v[24:25], v[16:17]\n\t"
        "v_pk_fma_f32 v[146:147], v[146:147], v[26:27], v[18:19]\n\t"
        "v_pk_fma_f32 v[148:149], v[148:149], v[28:29], v[20:21]\n\t"
        "v_pk_fma_f32 v[150:151], v[150:151], v[30:31], v[22:23]\n\t"
        "v_cvt_pk_bf16_f32 v40, v144, v145\n\t"
        "v_cvt_pk_bf16_f32 v41, v146, v147\n\t"
        "v_cvt_pk_bf16_f32 v42, v148, v149\n\t"
        "v_cvt_pk_bf16_f32 v43, v150, v151\n\t"
        "ds_write_b128 v12, v[40:43] offset:0\n\t"
        "v_pk_fma_f32 v[152:153], v[152:153], v[24:25], v[16:17]\n\t"
        "v_pk_fma_f32 v[154:155], v[154:155], v[26:27], v[18:19]\n\t"
        "v_pk_fma_f32 v[156:157], v[156:157], v[28:29], v[20:21]\n\t"
        "v_pk_fma_f32 v[158:159], v[158:159], v[30:31], v[22:23]\n\t"
        "v_cvt_pk_bf16_f32 v44, v152, v153\n\t"
        "v_cvt_pk_bf16_f32 v45, v154, v155\n\t"
        "v_cvt_pk_bf16_f32 v46, v156, v157\n\t"
        "v_cvt_pk_bf16_f32 v47, v158, v159\n\t"
        "ds_write_b128 v13, v[44:47] offset:0\n\t"
        "s_mov_b32 s4, 0xf149f2ca\n\t"
        "s_mov_b32 s5, 0x3eb8aa3b\n\t"
        "s_cmp_eq_u32 s29, 0\n\t"
        "s_cselect_b32 s6, s4, 0x7f7fffff\n\t"
        "v_mov_b32 v9, s4\n\t"
        "v_mov_b32 v8, 0\n\t"
        "v_lshl_add_u32 v6, v3, 2, 0\n\t"
        "v_sub_u32 v6, v4, v6\n\t"
        "v_and_b32 v7, 1, v6\n\t"
        "v_cmp_eq_u32 s[40:41], 0, v7\n\t"
        "v_cmp_le_i32 vcc, 0, v6\n\t"
        "s_nop 0\n\t"
        "s_and_b64 vcc, vcc, s[40:41]\n\t"
        "v_cndmask_b32 v48, v9, v8, vcc\n\t"
        "v_cmp_ge_i32 vcc, -2, v6\n\t"
        "s_nop 0\n\t"
        "s_and_b64 vcc, vcc, s[40:41]\n\t"
        "v_cndmask_b32 v32, v9, v8, vcc\n\t"
        "v_lshl_add_u32 v6, v3, 2, 1\n\t"
        "v_sub_u32 v6, v4, v6\n\t"
        "v_and_b32 v7, 1, v6\n\t"
        "v_cmp_eq_u32 s[40:41], 0, v7\n\t"
        "v_cmp_le_i32 vcc, 0, v6\n\t"
        "s_nop 0\n\t"
        "s_and_b64 vcc, vcc, s[40:41]\n\t"
        "v_cndmask_b32 v49, v9, v8, vcc\n\t"
        "v_cmp_ge_i32 vcc, -2, v6\n\t"
        "s_nop 0\n\t"
        "s_and_b64 vcc, vcc, s[40:41]\n\t"
        "v_cndmask_b32 v33, v9, v8, vcc\n\t"
        "v_lshl_add_u32 v6, v3, 2, 2\n\t"
        "v_sub_u32 v6, v4, v6\n\t"
        "v_and_b32 v7, 1, v6\n\t"
        "v_cmp_eq_u32 s[40:41], 0, v7\n\t"
        "v_cmp_le_i32 vcc, 0, v6\n\t"
        "s_nop 0\n\t"
        "s_and_b64 vcc, vcc, s[40:41]\n\t"
        "v_cndmask_b32 v50, v9, v8, vcc\n\t"
        "v_cmp_ge_i32 vcc, -2, v6\n\t"
        "s_nop 0\n\t"
        "s_and_b64 vcc, vcc, s[40:41]\n\t"
        "v_cndmask_b32 v34, v9, v8, vcc\n\t"
        "v_lshl_add_u32 v6, v3, 2, 3\n\t"
        "v_sub_u32 v6, v4, v6\n\t"
        "v_and_b32 v7, 1, v6\n\t"
        "v_cmp_eq_u32 s[40:41], 0, v7\n\t"
        "v_cmp_le_i32 vcc, 0, v6\n\t"
        "s_nop 0\n\t"
        "s_and_b64 vcc, vcc, s[40:41]\n\t"
        "v_cndmask_b32 v51, v9, v8, vcc\n\t"
        "v_cmp_ge_i32 vcc, -2, v6\n\t"
        "s_nop 0\n\t"
        "s_and_b64 vcc, vcc, s[40:41]\n\t"
        "v_cndmask_b32 v35, v9, v8, vcc\n\t"
        "s_waitcnt vmcnt(0)\n\t"
        "s_waitcnt lgkmcnt(0)\n\t"
        "s_barrier\n\t"
        "s_lshl_b32 s31, %2, 3\n\t"
        "s_lshl_b32 s33, s24, 12\n\t"
        "s_add_u32 s31, s31, s33\n\t"
        "s_add_u32 s31, s31, 655360\n\t"
        "v_lshlrev_b32 v6, 11, v1\n\t"
        "s_mov_b64 exec, 3\n\t"
        "buffer_store_dwordx2 v[14:15], v6, s[56:59], s31 offen sc1\n\t"
        "s_mov_b64 exec, -1\n\t"
        "ds_read_b128 v[76:79], v38 offset:0\n\t"
        "ds_read_b128 v[80:83], v39 offset:1024\n\t"
        "ds_read_b128 v[84:87], v38 offset:2048\n\t"
        "s_waitcnt lgkmcnt(2)\n\t"
        "v_mfma_f32_16x16x32_bf16 v[52:55], a[0:3], v[76:79], 0\n\t"
        "ds_read_b128 v[88:91], v39 offset:3072\n\t"
        "s_lshl_b32 s7, s27, 16\n\t"
        "s_lshl_b32 s33, s24, 14\n\t"
        "s_add_u32 s7, s7, s33\n\t"
        "v_mfma_f32_16x16x32_bf16 v[56:59], v[76:79], a[64:67], 0\n\t"
        "ds_read_b64_tr_b16 a[128:129], v5 offset:0\n\t"
        "ds_read_b64_tr_b16 a[130:131], v5 offset:512\n\t"
        "s_add_u32 s7, s7, 1048576\n\t"
        "s_lshl_b32 s8, s25, 11\n\t"
        "s_waitcnt lgkmcnt(4)\n\t"
        "v_mfma_f32_16x16x32_bf16 v[52:55], a[4:7], v[80:83], v[52:55]\n\t"
        "ds_read_b128 v[76:79], v38 offset:4096\n\t"
        "s_lshl_b32 s33, s24, 9\n\t"
        "s_add_u32 s8, s8, s33\n\t"
        "v_mfma_f32_16x16x32_bf16 v[56:59], v[80:83], a[68:71], v[56:59]\n\t"
        "ds_read_b64_tr_b16 a[132:133], v5 offset:4096\n\t"
        "ds_read_b64_tr_b16 a[134:135], v5 offset:4608\n\t"
        "s_add_u32 s8, s8, 655360\n\t"
        "v_lshlrev_b32 v129, 3, v1\n\t"
        "s_waitcnt lgkmcnt(6)\n\t"
        "v_mfma_f32_16x16x32_bf16 v[52:55], a[8:11], v[84:87], v[52:55]\n\t"
        "ds_read_b128 v[80:83], v39 offset:5120\n\t"
        "v_fma_f32 v160, v160, v24, v16\n\t"
        "v_fma_f32 v161, v161, v25, v17\n\t"
        "v_mfma_f32_16x16x32_bf16 v[56:59], v[84:87], a[72:75], v[56:59]\n\t"
        "ds_read_b64_tr_b16 a[136:137], v5 offset:8192\n\t"
        "ds_read_b64_tr_b16 a[138:139], v5 offset:8704\n\t"
        "v_fma_f32 v162, v162, v26, v18\n\t"
        "v_fma_f32 v163, v163, v27, v19\n\t"
        "v_fma_f32 v164, v164, v28, v20\n\t"
        "s_waitcnt lgkmcnt(8)\n\t"
        "v_mfma_f32_16x16x32_bf16 v[52:55], a[12:15], v[88:91], v[52:55]\n\t"
        "ds_read_b128 v[84:87], v38 offset:6144\n\t"
        "v_fma_f32 v165, v165, v29, v21\n\t"
        "v_fma_f32 v166, v166, v30, v22\n\t"
        "v_mfma_f32_16x16x32_bf16 v[56:59], v[88:91], a[76:79], v[56:59]\n\t"
        "ds_read_b64_tr_b16 a[140:141], v5 offset:12288\n\t"
        "ds_read_b64_tr_b16 a[142:143], v5 offset:12800\n\t"
        "v_fma_f32 v167, v167, v31, v23\n\t"
        "v_cvt_pk_bf16_f32 v40, v160, v161\n\t"
        "s_waitcnt lgkmcnt(8)\n\t"
        "v_mfma_f32_16x16x32_bf16 v[52:55], a[16:19], v[76:79], v[52:55]\n\t"
        "ds_read_b128 v[88:91], v39 offset:7168\n\t"
        "v_cvt_pk_bf16_f32 v41, v162, v163\n\t"
        "v_cvt_pk_bf16_f32 v42, v164, v165\n\t"
        "v_mfma_f32_16x16x32_bf16 v[56:59], v[76:79], a[80:83], v[56:59]\n\t"
        "ds_read_b64_tr_b16 a[144:145], v5 offset:16384\n\t"
        "ds_read_b64_tr_b16 a[146:147], v5 offset:16896\n\t"
        "v_cvt_pk_bf16_f32 v43, v166, v167\n\t"
        "ds_write_b128 v10, v[40:43] offset:16384\n\t"
        "s_waitcnt lgkmcnt(9)\n\t"
        "v_mfma_f32_16x16x32_bf16 v[52:55], a[20:23], v[80:83], v[52:55]\n\t"
        "ds_read_b128 v[76:79], v38 offset:8192\n\t"
        "v_fma_f32 v168, v168, v24, v16\n\t"
        "v_fma_f32 v169, v169, v25, v17\n\t"
        "v_fma_f32 v170, v170, v26, v18\n\t"
        "v_mfma_f32_16x16x32_bf16 v[56:59], v[80:83], a[84:87], v[56:59]\n\t"
        "ds_read_b64_tr_b16 a[148:149], v5 offset:20480\n\t"
        "ds_read_b64_tr_b16 a[150:151], v5 offset:20992\n\t"
        "v_fma_f32 v171, v171, v27, v19\n\t"
        "v_fma_f32 v172, v172, v28, v20\n\t"
        "s_waitcnt lgkmcnt(9)\n\t"
        "v_mfma_f32_16x16x32_bf16 v[52:55], a[24:27], v[84:87], v[52:55]\n\t"
        "ds_read_b128 v[80:83], v39 offset:9216\n\t"
        "v_fma_f32 v173, v173, v29, v21\n\t"
        "v_fma_f32 v174, v174, v30, v22\n\t"
        "v_mfma_f32_16x16x32_bf16 v[56:59], v[84:87], a[88:91], v[56:59]\n\t"
        "ds_read_b64_tr_b16 a[152:153], v5 offset:24576\n\t"
        "ds_read_b64_tr_b16 a[154:155], v5 offset:25088\n\t"
        "v_fma_f32 v175, v175, v31, v23\n\t"
        "v_cvt_pk_bf16_f32 v44, v168, v169\n\t"
        "s_waitcnt lgkmcnt(9)\n\t"
        "v_mfma_f32_16x16x32_bf16 v[52:55], a[28:31], v[88:91], v[52:55]\n\t"
        "ds_read_b128 v[84:87], v38 offset:10240\n\t"
        "v_cvt_pk_bf16_f32 v45, v170, v171\n\t"
        "v_cvt_pk_bf16_f32 v46, v172, v173\n\t"
        "v_mfma_f32_16x16x32_bf16 v[56:59], v[88:91], a[92:95], v[56:59]\n\t"
        "ds_read_b64_tr_b16 a[156:157], v5 offset:28672\n\t"
        "ds_read_b64_tr_b16 a[158:159], v5 offset:29184\n\t"
        "v_cvt_pk_bf16_f32 v47, v174, v175\n\t"
        "ds_write_b128 v11, v[44:47] offset:16384\n\t"
        "s_waitcnt lgkmcnt(0)\n\t"
        "s_barrier\n\t"
        "v_mfma_f32_16x16x32_bf16 v[52:55], a[32:35], v[76:79], v[52:55]\n\t"
        "ds_read_b128 v[88:91], v39 offset:11264\n\t"
        "v_fma_f32 v176, v176, v24, v16\n\t"
        "v_fma_f32 v177, v177, v25, v17\n\t"
        "v_mfma_f32_16x16x32_bf16 v[56:59], v[76:79], a[96:99], v[56:59]\n\t"
        "ds_read_b64_tr_b16 a[160:161], v5 offset:32768\n\t"
        "ds_read_b64_tr_b16 a[162:163], v5 offset:33280\n\t"
        "v_fma_f32 v178, v178, v26, v18\n\t"
        "v_fma_f32 v179, v179, v27, v19\n\t"
        "v_mfma_f32_16x16x32_bf16 v[52:55], a[36:39], v[80:83], v[52:55]\n\t"
        "ds_read_b128 v[76:79], v38 offset:12288\n\t"
        "v_fma_f32 v180, v180, v28, v20\n\t"
        "v_mfma_f32_16x16x32_bf16 v[56:59], v[80:83], a[100:103], v[56:59]\n\t"
        "ds_read_b64_tr_b16 a[164:165], v5 offset:36864\n\t"
        "ds_read_b64_tr_b16 a[166:167], v5 offset:37376\n\t"
        "v_fma_f32 v181, v181, v29, v21\n\t"
        "v_fma_f32 v182, v182, v30, v22\n\t"
        "v_mfma_f32_16x16x32_bf16 v[52:55], a[40:43], v[84:87], v[52:55]\n\t"
        "ds_read_b128 v[80:83], v39 offset:13312\n\t"
        "v_fma_f32 v183, v183, v31, v23\n\t"
        "v_cvt_pk_bf16_f32 v40, v176, v177\n\t"
        "v_mfma_f32_16x16x32_bf16 v[56:59], v[84:87], a[104:107], v[56:59]\n\t"
        "ds_read_b64_tr_b16 a[168:169], v5 offset:40960\n\t"
        "ds_read_b64_tr_b16 a[170:171], v5 offset:41472\n\t"
        "v_cvt_pk_bf16_f32 v41, v178, v179\n\t"
        "s_waitcnt lgkmcnt(8)\n\t"
        "v_mfma_f32_16x16x32_bf16 v[52:55], a[44:47], v[88:91], v[52:55]\n\t"
        "ds_read_b128 v[84:87], v38 offset:14336\n\t"
        "v_cvt_pk_bf16_f32 v42, v180, v181\n\t"
        "v_cvt_pk_bf16_f32 v43, v182, v183\n\t"
        "v_mfma_f32_16x16x32_bf16 v[56:59], v[88:91], a[108:111], v[56:59]\n\t"
        "ds_read_b64_tr_b16 a[172:173], v5 offset:45056\n\t"
        "ds_read_b64_tr_b16 a[174:175], v5 offset:45568\n\t"
        "ds_write_b128 v12, v[40:43] offset:16384\n\t"
        "s_waitcnt lgkmcnt(9)\n\t"
        "v_mfma_f32_16x16x32_bf16 v[52:55], a[48:51], v[76:79], v[52:55]\n\t"
        "ds_read_b128 v[88:91], v39 offset:15360\n\t"
        "v_fma_f32 v184, v184, v24, v16\n\t"
        "v_fma_f32 v185, v185, v25, v17\n\t"
        "v_mfma_f32_16x16x32_bf16 v[56:59], v[76:79], a[112:115], v[56:59]\n\t"
        "ds_read_b64_tr_b16 a[176:177], v5 offset:49152\n\t"
        "ds_read_b64_tr_b16 a[178:179], v5 offset:49664\n\t"
        "v_fma_f32 v186, v186, v26, v18\n\t"
        "v_fma_f32 v187, v187, v27, v19\n\t"
        "s_waitcnt lgkmcnt(9)\n\t"
        "v_mfma_f32_16x16x32_bf16 v[52:55], a[52:55], v[80:83], v[52:55]\n\t"
        "ds_read_b128 v[76:79], v36 offset:0\n\t"
        "v_fma_f32 v188, v188, v28, v20\n\t"
        "v_mfma_f32_16x16x32_bf16 v[56:59], v[80:83], a[116:119], v[56:59]\n\t"
        "ds_read_b64_tr_b16 a[180:181], v5 offset:53248\n\t"
        "ds_read_b64_tr_b16 a[182:183], v5 offset:53760\n\t"
        "v_fma_f32 v189, v189, v29, v21\n\t"
        "v_fma_f32 v190, v190, v30, v22\n\t"
        "s_waitcnt lgkmcnt(9)\n\t"
        "v_mfma_f32_16x16x32_bf16 v[52:55], a[56:59], v[84:87], v[52:55]\n\t"
        "ds_read_b128 v[80:83], v37 offset:1024\n\t"
        "v_fma_f32 v191, v191, v31, v23\n\t"
        "v_cvt_pk_bf16_f32 v44, v184, v185\n\t"
        "v_mfma_f32_16x16x32_bf16 v[56:59], v[84:87], a[120:123], v[56:59]\n\t"
        "ds_read_b64_tr_b16 a[184:185], v5 offset:57344\n\t"
        "ds_read_b64_tr_b16 a[186:187], v5 offset:57856\n\t"
        "v_cvt_pk_bf16_f32 v45, v186, v187\n\t"
        "s_waitcnt lgkmcnt(8)\n\t"
        "v_mfma_f32_16x16x32_bf16 v[52:55], a[60:63], v[88:91], v[52:55]\n\t"
        "ds_read_b128 v[84:87], v36 offset:2048\n\t"
        "v_cvt_pk_bf16_f32 v46, v188, v189\n\t"
        "v_cvt_pk_bf16_f32 v47, v190, v191\n\t"
        "v_mfma_f32_16x16x32_bf16 v[56:59], v[88:91], a[124:127], v[56:59]\n\t"
        "ds_read_b64_tr_b16 a[188:189], v5 offset:61440\n\t"
        "ds_read_b64_tr_b16 a[190:191], v5 offset:61952\n\t"
        "ds_write_b128 v13, v[44:47] offset:16384\n\t"
        "s_waitcnt lgkmcnt(9)\n\t"
        "v_mfma_f32_16x16x32_bf16 v[64:67], a[0:3], v[76:79], 0\n\t"
        "ds_read_b128 v[88:91], v37 offset:3072\n\t"
        "v_cvt_pk_bf16_f32 v92, v52, v53\n\t"
        "v_fma_f32 v192, v192, v24, v16\n\t"
        "v_mfma_f32_16x16x32_bf16 v[68:71], v[76:79], a[64:67], 0\n\t"
        "v_cvt_pk_bf16_f32 v93, v54, v55\n\t"
        "v_fma_f32 v193, v193, v25, v17\n\t"
        "v_cvt_pk_bf16_f32 v94, v56, v57\n\t"
        "v_mfma_f32_16x16x32_bf16 v[72:75], a[128:131], v[76:79], 0\n\t"
        "v_fma_f32 v194, v194, v26, v18\n\t"
        "v_cvt_pk_bf16_f32 v95, v58, v59\n\t"
        "v_fma_f32 v195, v195, v27, v19\n\t"
        "s_waitcnt lgkmcnt(7)\n\t"
        "v_mfma_f32_16x16x32_bf16 v[64:67], a[4:7], v[80:83], v[64:67]\n\t"
        "ds_read_b128 v[76:79], v36 offset:4096\n\t"
        "s_lshl_b32 s31, s27, 2\n\t"
        "v_fma_f32 v196, v196, v28, v20\n\t"
        "v_mfma_f32_16x16x32_bf16 v[68:71], v[80:83], a[68:71], v[68:71]\n\t"
        "s_add_u32 s31, s31, s24\n\t"
        "v_fma_f32 v197, v197, v29, v21\n\t"
        "s_mul_i32 s33, s31, 3\n\t"
        "v_mfma_f32_16x16x32_bf16 v[72:75], a[132:135], v[80:83], v[72:75]\n\t"
        "v_fma_f32 v198, v198, v30, v22\n\t"
        "s_lshl_b32 s36, s31, 1\n\t"
        "v_fma_f32 v199, v199, v31, v23\n\t"
        "s_waitcnt lgkmcnt(5)\n\t"
        "v_mfma_f32_16x16x32_bf16 v[64:67], a[8:11], v[84:87], v[64:67]\n\t"
        "ds_read_b128 v[80:83], v37 offset:5120\n\t"
        "s_add_u32 s36, s36, 22\n\t"
        "v_cvt_pk_bf16_f32 v40, v192, v193\n\t"
        "v_mfma_f32_16x16x32_bf16 v[68:71], v[84:87], a[72:75], v[68:71]\n\t"
        "s_cmp_lt_u32 s31, 22\n\t"
        "v_cvt_pk_bf16_f32 v41, v194, v195\n\t"
        "s_cselect_b32 s9, s33, s36\n\t"
        "v_mfma_f32_16x16x32_bf16 v[72:75], a[136:139], v[84:87], v[72:75]\n\t"
        "v_cvt_pk_bf16_f32 v42, v196, v197\n\t"
        "s_cselect_b32 s10, 3, 2\n\t"
        "v_cvt_pk_bf16_f32 v43, v198, v199\n\t"
        "s_waitcnt lgkmcnt(2)\n\t"
        "v_mfma_f32_16x16x32_bf16 v[64:67], a[12:15], v[88:91], v[64:67]\n\t"
        "ds_read_b128 v[84:87], v36 offset:6144\n\t"
        "s_lshl_b32 s11, s9, 14\n\t"
        "ds_write_b128 v10, v[40:43] offset:32768\n\t"
        "v_mfma_f32_16x16x32_bf16 v[68:71], v[88:91], a[76:79], v[68:71]\n\t"
        "s_add_u32 s11, s11, 1572864\n\t"
        "v_fma_f32 v200, v200, v24, v16\n\t"
        "s_add_u32 s12, s11, 16384\n\t"
        "v_mfma_f32_16x16x32_bf16 v[72:75], a[140:143], v[88:91], v[72:75]\n\t"
        "v_fma_f32 v201, v201, v25, v17\n\t"
        "s_add_u32 s13, s11, 32768\n\t"
        "v_fma_f32 v202, v202, v26, v18\n\t"
        "s_waitcnt lgkmcnt(3)\n\t"
        "v_mfma_f32_16x16x32_bf16 v[64:67], a[16:19], v[76:79], v[64:67]\n\t"
        "ds_read_b128 v[88:91], v37 offset:7168\n\t"
        "s_cmp_eq_u32 s10, 3\n\t"
        "v_fma_f32 v203, v203, v27, v19\n\t"
        "v_mfma_f32_16x16x32_bf16 v[68:71], v[76:79], a[80:83], v[68:71]\n\t"
        "s_cselect_b32 s13, s13, s11\n\t"
        "v_fma_f32 v204, v204, v28, v20\n\t"
        "v_and_b32 v6, 1, v3\n\t"
        "v_mfma_f32_16x16x32_bf16 v[72:75], a[144:147], v[76:79], v[72:75]\n\t"
        "v_fma_f32 v205, v205, v29, v21\n\t"
        "v_lshrrev_b32 v7, 1, v3\n\t"
        "v_fma_f32 v206, v206, v30, v22\n\t"
        "s_waitcnt lgkmcnt(3)\n\t"
        "v_mfma_f32_16x16x32_bf16 v[64:67], a[20:23], v[80:83], v[64:67]\n\t"
        "ds_read_b128 v[76:79], v36 offset:8192\n\t"
        "v_lshlrev_b32 v8, 5, v6\n\t"
        "v_mfma_f32_16x16x32_bf16 v[68:71], v[80:83], a[84:87], v[68:71]\n\t"
        "v_fma_f32 v207, v207, v31, v23\n\t"
        "v_lshl_add_u32 v8, v7, 4, v8\n\t"
        "v_cvt_pk_bf16_f32 v44, v200, v201\n\t"
        "v_lshlrev_b32 v140, 1, v8\n\t"
        "v_mfma_f32_16x16x32_bf16 v[72:75], a[148:151], v[80:83], v[72:75]\n\t"
        "v_cvt_pk_bf16_f32 v45, v202, v203\n\t"
        "v_lshl_add_u32 v5, v4, 10, v8\n\t"
        "v_cvt_pk_bf16_f32 v46, v204, v205\n\t"
        "s_waitcnt lgkmcnt(3)\n\t"
        "v_mfma_f32_16x16x32_bf16 v[64:67], a[24:27], v[84:87], v[64:67]\n\t"
        "ds_read_b128 v[80:83], v37 offset:9216\n\t"
        "v_lshl_add_u32 v6, v6, 1, v7\n\t"
        "v_mfma_f32_16x16x32_bf16 v[68:71], v[84:87], a[88:91], v[68:71]\n\t"
        "v_cvt_pk_bf16_f32 v47, v206, v207\n\t"
        "v_lshl_add_u32 v6, v6, 4, v4\n\t"
        "ds_write_b128 v11, v[44:47] offset:32768\n\t"
        "v_mfma_f32_16x16x32_bf16 v[72:75], a[152:155], v[84:87], v[72:75]\n\t"
        "v_lshlrev_b32 v141, 4, v6\n\t"
        "v_fma_f32 v208, v208, v24, v16\n\t"
        "s_lshl_b32 s38, s24, 4\n\t"
        "v_fma_f32 v209, v209, v25, v17\n\t"
        "s_waitcnt lgkmcnt(3)\n\t"
        "v_mfma_f32_16x16x32_bf16 v[64:67], a[28:31], v[88:91], v[64:67]\n\t"
        "ds_read_b128 v[84:87], v36 offset:10240\n\t"
        "s_add_u32 s38, s38, s30\n\t"
        "v_mfma_f32_16x16x32_bf16 v[68:71], v[88:91], a[92:95], v[68:71]\n\t"
        "v_fma_f32 v210, v210, v26, v18\n\t"
        "s_lshl_b32 s14, s38, 10\n\t"
        "v_fma_f32 v211, v211, v27, v19\n\t"
        "v_mfma_f32_16x16x32_bf16 v[72:75], a[156:159], v[88:91], v[72:75]\n\t"
        "s_lshl_b32 s38, s27, 7\n\t"
        "v_fma_f32 v212, v212, v28, v20\n\t"
        "s_add_u32 s14, s14, s38\n\t"
        "s_waitcnt lgkmcnt(0)\n\t"
        "s_barrier\n\t"
        "v_mfma_f32_16x16x32_bf16 v[64:67], a[32:35], v[76:79], v[64:67]\n\t"
        "ds_read_b128 v[88:91], v37 offset:11264\n\t"
        "v_fma_f32 v213, v213, v29, v21\n\t"
        "buffer_load_dwordx4 v[144:147], v5, s[44:47], s14 offen\n\t"
        "v_mfma_f32_16x16x32_bf16 v[68:71], v[76:79], a[96:99], v[68:71]\n\t"
        "v_fma_f32 v214, v214, v30, v22\n\t"
        "buffer_load_dwordx4 v[148:151], v5, s[44:47], s14 offen offset:64\n\t"
        "v_mfma_f32_16x16x32_bf16 v[72:75], a[160:163], v[76:79], v[72:75]\n\t"
        "v_fma_f32 v215, v215, v31, v23\n\t"
        "v_cvt_pk_bf16_f32 v40, v208, v209\n\t"
        "v_cvt_pk_bf16_f32 v41, v210, v211\n\t"
        "v_mfma_f32_16x16x32_bf16 v[64:67], a[36:39], v[80:83], v[64:67]\n\t"
        "ds_read_b128 v[76:79], v36 offset:12288\n\t"
        "v_cvt_pk_bf16_f32 v42, v212, v213\n\t"
        "v_mfma_f32_16x16x32_bf16 v[68:71], v[80:83], a[100:103], v[68:71]\n\t"
        "v_cvt_pk_bf16_f32 v43, v214, v215\n\t"
        "ds_write_b128 v12, v[40:43] offset:32768\n\t"
        "v_mfma_f32_16x16x32_bf16 v[72:75], a[164:167], v[80:83], v[72:75]\n\t"
        "v_fma_f32 v216, v216, v24, v16\n\t"
        "v_fma_f32 v217, v217, v25, v17\n\t"
        "v_fma_f32 v218, v218, v26, v18\n\t"
        "v_mfma_f32_16x16x32_bf16 v[64:67], a[40:43], v[84:87], v[64:67]\n\t"
        "ds_read_b128 v[80:83], v37 offset:13312\n\t"
        "v_fma_f32 v219, v219, v27, v19\n\t"
        "v_mfma_f32_16x16x32_bf16 v[68:71], v[84:87], a[104:107], v[68:71]\n\t"
        "v_fma_f32 v220, v220, v28, v20\n\t"
        "v_fma_f32 v221, v221, v29, v21\n\t"
        "v_fma_f32 v222, v222, v30, v22\n\t"
        "v_mfma_f32_16x16x32_bf16 v[72:75], a[168:171], v[84:87], v[72:75]\n\t"
        "v_fma_f32 v223, v223, v31, v23\n\t"
        "v_cvt_pk_bf16_f32 v44, v216, v217\n\t"
        "s_waitcnt lgkmcnt(3)\n\t"
        "v_mfma_f32_16x16x32_bf16 v[64:67], a[44:47], v[88:91], v[64:67]\n\t"
        "ds_read_b128 v[84:87], v36 offset:14336\n\t"
        "v_cvt_pk_bf16_f32 v45, v218, v219\n\t"
        "v_mfma_f32_16x16x32_bf16 v[68:71], v[88:91], a[108:111], v[68:71]\n\t"
        "v_cvt_pk_bf16_f32 v46, v220, v221\n\t"
        "v_cvt_pk_bf16_f32 v47, v222, v223\n\t"
        "ds_write_b128 v13, v[44:47] offset:32768\n\t"
        "v_mfma_f32_16x16x32_bf16 v[72:75], a[172:175], v[88:91], v[72:75]\n\t"
        "v_fma_f32 v224, v224, v24, v16\n\t"
        "v_fma_f32 v225, v225, v25, v17\n\t"
        "s_waitcnt lgkmcnt(4)\n\t"
        "v_mfma_f32_16x16x32_bf16 v[64:67], a[48:51], v[76:79], v[64:67]\n\t"
        "ds_read_b128 v[88:91], v37 offset:15360\n\t"
        "v_fma_f32 v226, v226, v26, v18\n\t"
        "v_fma_f32 v227, v227, v27, v19\n\t"
        "v_mfma_f32_16x16x32_bf16 v[68:71], v[76:79], a[112:115], v[68:71]\n\t"
        "v_fma_f32 v228, v228, v28, v20\n\t"
        "v_fma_f32 v229, v229, v29, v21\n\t"
        "v_mfma_f32_16x16x32_bf16 v[72:75], a[176:179], v[76:79], v[72:75]\n\t"
        "v_fma_f32 v230, v230, v30, v22\n\t"
        "v_fma_f32 v231, v231, v31, v23\n\t"
        "v_cvt_pk_bf16_f32 v40, v224, v225\n\t"
        "s_waitcnt lgkmcnt(3)\n\t"
        "v_mfma_f32_16x16x32_bf16 v[64:67], a[52:55], v[80:83], v[64:67]\n\t"
        "ds_read_b128 v[76:79], v36 offset:16384\n\t"
        "v_cvt_pk_bf16_f32 v41, v226, v227\n\t"
        "v_mfma_f32_16x16x32_bf16 v[68:71], v[80:83], a[116:119], v[68:71]\n\t"
        "v_cvt_pk_bf16_f32 v42, v228, v229\n\t"
        "v_cvt_pk_bf16_f32 v43, v230, v231\n\t"
        "v_mfma_f32_16x16x32_bf16 v[72:75], a[180:183], v[80:83], v[72:75]\n\t"
        "ds_write_b128 v10, v[40:43] offset:49152\n\t"
        "v_fma_f32 v232, v232, v24, v16\n\t"
        "v_fma_f32 v233, v233, v25, v17\n\t"
        "s_waitcnt lgkmcnt(4)\n\t"
        "v_mfma_f32_16x16x32_bf16 v[64:67], a[56:59], v[84:87], v[64:67]\n\t"
        "ds_read_b128 v[80:83], v37 offset:17408\n\t"
        "v_fma_f32 v234, v234, v26, v18\n\t"
        "v_mfma_f32_16x16x32_bf16 v[68:71], v[84:87], a[120:123], v[68:71]\n\t"
        "v_fma_f32 v235, v235, v27, v19\n\t"
        "v_fma_f32 v236, v236, v28, v20\n\t"
        "v_fma_f32 v237, v237, v29, v21\n\t"
        "v_mfma_f32_16x16x32_bf16 v[72:75], a[184:187], v[84:87], v[72:75]\n\t"
        "v_fma_f32 v238, v238, v30, v22\n\t"
        "v_fma_f32 v239, v239, v31, v23\n\t"
        "s_waitcnt lgkmcnt(3)\n\t"
        "v_mfma_f32_16x16x32_bf16 v[64:67], a[60:63], v[88:91], v[64:67]\n\t"
        "ds_read_b128 v[84:87], v36 offset:18432\n\t"
        "v_cvt_pk_bf16_f32 v44, v232, v233\n\t"
        "v_mfma_f32_16x16x32_bf16 v[68:71], v[88:91], a[124:127], v[68:71]\n\t"
        "v_cvt_pk_bf16_f32 v45, v234, v235\n\t"
        "v_cvt_pk_bf16_f32 v46, v236, v237\n\t"
        "v_cvt_pk_bf16_f32 v47, v238, v239\n\t"
        "v_mfma_f32_16x16x32_bf16 v[72:75], a[188:191], v[88:91], v[72:75]\n\t"
        "ds_write_b128 v11, v[44:47] offset:49152\n\t"
        "buffer_load_dwordx2 v[130:131], v129, s[56:59], s8 offen sc1\n\t"
        "s_waitcnt lgkmcnt(4)\n\t"
        "v_mfma_f32_16x16x32_bf16 v[52:55], a[0:3], v[76:79], 0\n\t"
        "ds_read_b128 v[88:91], v37 offset:19456\n\t"
        "v_cvt_pk_bf16_f32 v96, v64, v65\n\t"
        "v_fma_f32 v240, v240, v24, v16\n\t"
        "v_mfma_f32_16x16x32_bf16 v[56:59], v[76:79], a[64:67], 0\n\t"
        "v_cvt_pk_bf16_f32 v97, v66, v67\n\t"
        "v_fma_f32 v241, v241, v25, v17\n\t"
        "v_cvt_pk_bf16_f32 v100, v72, v73\n\t"
        "v_mfma_f32_16x16x32_bf16 v[60:63], a[128:131], v[76:79], 0\n\t"
        "v_fma_f32 v242, v242, v26, v18\n\t"
        "v_cvt_pk_bf16_f32 v101, v74, v75\n\t"
        "v_fma_f32 v243, v243, v27, v19\n\t"
        "s_waitcnt lgkmcnt(3)\n\t"
        "v_mfma_f32_16x16x32_bf16 v[52:55], a[4:7], v[80:83], v[52:55]\n\t"
        "ds_read_b128 v[76:79], v36 offset:20480\n\t"
        "v_cvt_pk_bf16_f32 v98, v68, v69\n\t"
        "v_fma_f32 v244, v244, v28, v20\n\t"
        "v_mfma_f32_16x16x32_bf16 v[56:59], v[80:83], a[68:71], v[56:59]\n\t"
        "v_cvt_pk_bf16_f32 v99, v70, v71\n\t"
        "v_fma_f32 v245, v245, v29, v21\n\t"
        "v_mfma_f32_16x16x16_bf16 v[112:115], v[92:93], v[100:101], 0\n\t"
        "v_mfma_f32_16x16x32_bf16 v[60:63], a[132:135], v[80:83], v[60:63]\n\t"
        "v_fma_f32 v246, v246, v30, v22\n\t"
        "v_mfma_f32_16x16x16_bf16 v[116:119], v[96:97], v[100:101], 0\n\t"
        "v_fma_f32 v247, v247, v31, v23\n\t"
        "s_waitcnt lgkmcnt(3)\n\t"
        "v_mfma_f32_16x16x32_bf16 v[52:55], a[8:11], v[84:87], v[52:55]\n\t"
        "ds_read_b128 v[80:83], v37 offset:21504\n\t"
        "v_cvt_pk_bf16_f32 v40, v240, v241\n\t"
        "v_cvt_pk_bf16_f32 v41, v242, v243\n\t"
        "v_mfma_f32_16x16x32_bf16 v[56:59], v[84:87], a[72:75], v[56:59]\n\t"
        "v_fma_f32 v104, v112, s5, v32\n\t"
        "v_cvt_pk_bf16_f32 v42, v244, v245\n\t"
        "v_fma_f32 v105, v113, s5, v33\n\t"
        "v_mfma_f32_16x16x32_bf16 v[60:63], a[136:139], v[84:87], v[60:63]\n\t"
        "v_cvt_pk_bf16_f32 v43, v246, v247\n\t"
        "v_fma_f32 v106, v114, s5, v34\n\t"
        "ds_write_b128 v12, v[40:43] offset:49152\n\t"
        "s_waitcnt lgkmcnt(3)\n\t"
        "v_mfma_f32_16x16x32_bf16 v[52:55], a[12:15], v[88:91], v[52:55]\n\t"
        "ds_read_b128 v[84:87], v36 offset:22528\n\t"
        "v_fma_f32 v107, v115, s5, v35\n\t"
        "v_fma_f32 v248, v248, v24, v16\n\t"
        "v_mfma_f32_16x16x32_bf16 v[56:59], v[88:91], a[76:79], v[56:59]\n\t"
        "v_min_f32 v104, s6, v104\n\t"
        "v_fma_f32 v249, v249, v25, v17\n\t"
        "v_min_f32 v105, s6, v105\n\t"
        "v_mfma_f32_16x16x32_bf16 v[60:63], a[140:143], v[88:91], v[60:63]\n\t"
        "v_fma_f32 v250, v250, v26, v18\n\t"
        "v_min_f32 v106, s6, v106\n\t"
        "v_fma_f32 v251, v251, v27, v19\n\t"
        "s_waitcnt lgkmcnt(3)\n\t"
        "v_mfma_f32_16x16x32_bf16 v[52:55], a[16:19], v[76:79], v[52:55]\n\t"
        "ds_read_b128 v[88:91], v37 offset:23552\n\t"
        "v_min_f32 v107, s6, v107\n\t"
        "v_fma_f32 v252, v252, v28, v20\n\t"
        "v_mfma_f32_16x16x32_bf16 v[56:59], v[76:79], a[80:83], v[56:59]\n\t"
        "v_fma_f32 v108, v116, s5, v48\n\t"
        "v_fma_f32 v253, v253, v29, v21\n\t"
        "v_fma_f32 v109, v117, s5, v49\n\t"
        "v_mfma_f32_16x16x32_bf16 v[60:63], a[144:147], v[76:79], v[60:63]\n\t"
        "v_fma_f32 v254, v254, v30, v22\n\t"
        "v_fma_f32 v110, v118, s5, v50\n\t"
        "v_fma_f32 v255, v255, v31, v23\n\t"
        "s_waitcnt lgkmcnt(3)\n\t"
        "v_mfma_f32_16x16x32_bf16 v[52:55], a[20:23], v[80:83], v[52:55]\n\t"
        "ds_read_b128 v[76:79], v36 offset:24576\n\t"
        "v_fma_f32 v111, v119, s5, v51\n\t"
        "v_cvt_pk_bf16_f32 v44, v248, v249\n\t"
        "v_mfma_f32_16x16x32_bf16 v[56:59], v[80:83], a[84:87], v[56:59]\n\t"
        "v_max3_f32 v102, v104, v105, v106\n\t"
        "v_cvt_pk_bf16_f32 v45, v250, v251\n\t"
        "v_max3_f32 v103, v107, v108, v109\n\t"
        "v_mfma_f32_16x16x32_bf16 v[60:63], a[148:151], v[80:83], v[60:63]\n\t"
        "v_cvt_pk_bf16_f32 v46, v252, v253\n\t"
        "v_max3_f32 v102, v110, v111, v102\n\t"
        "v_cvt_pk_bf16_f32 v47, v254, v255\n\t"
        "s_waitcnt lgkmcnt(2)\n\t"
        "v_mfma_f32_16x16x32_bf16 v[52:55], a[24:27], v[84:87], v[52:55]\n\t"
        "ds_read_b128 v[80:83], v37 offset:25600\n\t"
        "v_mov_b32 v112, v94\n\t"
        "ds_write_b128 v13, v[44:47] offset:49152\n\t"
        "v_mfma_f32_16x16x32_bf16 v[56:59], v[84:87], a[88:91], v[56:59]\n\t"
        "v_max_f32 v102, v102, v103\n\t"
        "v_mov_b32 v103, v102\n\t"
        "v_mov_b32 v113, v95\n\t"
        "v_mfma_f32_16x16x32_bf16 v[60:63], a[152:155], v[84:87], v[60:63]\n\t"
        "v_permlane16_swap_b32 v102, v103\n\t"
        "v_max_f32 v102, v102, v103\n\t"
        "v_mov_b32 v103, v102\n\t"
        "s_waitcnt lgkmcnt(3)\n\t"
        "v_mfma_f32_16x16x32_bf16 v[52:55], a[28:31], v[88:91], v[52:55]\n\t"
        "ds_read_b128 v[84:87], v36 offset:26624\n\t"
        "v_mov_b32 v114, v98\n\t"
        "v_permlane32_swap_b32 v102, v103\n\t"
        "v_mfma_f32_16x16x32_bf16 v[56:59], v[88:91], a[92:95], v[56:59]\n\t"
        "v_max_f32 v102, v102, v103\n\t"
        "v_mov_b32 v115, v99\n\t"
        "v_sub_f32 v104, v104, v102\n\t"
        "v_mfma_f32_16x16x32_bf16 v[60:63], a[156:159], v[88:91], v[60:63]\n\t"
        "v_sub_f32 v105, v105, v102\n\t"
        "v_sub_f32 v106, v106, v102\n\t"
        "v_sub_f32 v107, v107, v102\n\t"
        "s_waitcnt vmcnt(0)\n\t"
        "v_cmp_ne_u64 vcc, s[34:35], v[130:131]\n\t"
        "s_nop 0\n\t"
        "s_cbranch_vccz .Lppok8\n\t"
        "s_mov_b32 s31, 65536\n\t"
        ".Lppsp9:\n\t"
        "buffer_load_dwordx2 v[130:131], v129, s[56:59], s8 offen sc1\n\t"
        "s_waitcnt vmcnt(0)\n\t"
        "v_cmp_ne_u64 vcc, s[34:35], v[130:131]\n\t"
        "s_nop 0\n\t"
        "s_cbranch_vccz .Lppok8\n\t"
        "s_sub_u32 s31, s31, 1\n\t"
        "s_cmp_lg_u32 s31, 0\n\t"
        "s_cbranch_scc1 .Lppsp9\n\t"
        ".Lppok8:\n\t"
        "s_waitcnt lgkmcnt(0)\n\t"
        "s_barrier\n\t"
        "s_cmp_lg_u32 %2, 0\n\t"
        "s_cbranch_scc1 .Lep10\n\t"
        "s_cmp_lg_u32 s24, 0\n\t"
        "s_cbranch_scc1 .Lep10\n\t"
        "v_mov_b32 v130, 0\n\t"
        "s_mov_b64 exec, 1\n\t"
        "buffer_store_dwordx2 v[14:15], v130, s[56:59], 0 offen sc1\n\t"
        "s_mov_b64 exec, -1\n\t"
        ".Lep10:\n\t"
        "s_nop 1\n\t"
        "v_mfma_f32_16x16x32_bf16 v[52:55], a[32:35], v[76:79], v[52:55]\n\t"
        "ds_read_b128 v[88:91], v37 offset:27648\n\t"
        "v_sub_f32 v108, v108, v102\n\t"
        "buffer_load_dwordx4 a[192:195], v2, s[56:59], s7 offen offset:0 sc1\n\t"
        "v_mfma_f32_16x16x32_bf16 v[56:59], v[76:79], a[96:99], v[56:59]\n\t"
        "v_sub_f32 v109, v109, v102\n\t"
        "buffer_load_dwordx4 a[196:199], v2, s[56:59], s7 offen offset:1024 sc1\n\t"
        "v_mfma_f32_16x16x32_bf16 v[60:63], a[160:163], v[76:79], v[60:63]\n\t"
        "v_sub_f32 v110, v110, v102\n\t"
        "buffer_load_dwordx4 a[200:203], v2, s[56:59], s7 offen offset:2048 sc1\n\t"
        "v_mfma_f32_16x16x32_bf16 v[52:55], a[36:39], v[80:83], v[52:55]\n\t"
        "ds_read_b128 v[76:79], v36 offset:28672\n\t"
        "v_sub_f32 v111, v111, v102\n\t"
        "buffer_load_dwordx4 a[204:207], v2, s[56:59], s7 offen offset:3072 sc1\n\t"
        "v_mfma_f32_16x16x32_bf16 v[56:59], v[80:83], a[100:103], v[56:59]\n\t"
        "v_exp_f32 v104, v104\n\t"
        "s_add_u32 s7, s7, 4096\n\t"
        "v_mfma_f32_16x16x32_bf16 v[60:63], a[164:167], v[80:83], v[60:63]\n\t"
        "v_exp_f32 v105, v105\n\t"
        "buffer_load_dwordx4 a[208:211], v2, s[56:59], s7 offen offset:0 sc1\n\t"
        "v_mfma_f32_16x16x32_bf16 v[52:55], a[40:43], v[84:87], v[52:55]\n\t"
        "ds_read_b128 v[80:83], v37 offset:29696\n\t"
        "v_exp_f32 v106, v106\n\t"
        "v_mfma_f32_16x16x32_bf16 v[56:59], v[84:87], a[104:107], v[56:59]\n\t"
        "buffer_load_dwordx4 a[212:215], v2, s[56:59], s7 offen offset:1024 sc1\n\t"
        "v_exp_f32 v107, v107\n\t"
        "buffer_load_dwordx4 a[216:219], v2, s[56:59], s7 offen offset:2048 sc1\n\t"
        "v_mfma_f32_16x16x32_bf16 v[60:63], a[168:171], v[84:87], v[60:63]\n\t"
        "v_exp_f32 v108, v108\n\t"
        "buffer_load_dwordx4 a[220:223], v2, s[56:59], s7 offen offset:3072 sc1\n\t"
        "s_waitcnt lgkmcnt(2)\n\t"
        "v_mfma_f32_16x16x32_bf16 v[52:55], a[44:47], v[88:91], v[52:55]\n\t"
        "ds_read_b128 v[84:87], v36 offset:30720\n\t"
        "v_exp_f32 v109, v109\n\t"
        "v_mfma_f32_16x16x32_bf16 v[56:59], v[88:91], a[108:111], v[56:59]\n\t"
        "s_add_u32 s7, s7, 4096\n\t"
        "v_exp_f32 v110, v110\n\t"
        "v_exp_f32 v111, v111\n\t"
        "v_mfma_f32_16x16x32_bf16 v[60:63], a[172:175], v[88:91], v[60:63]\n\t"
        "v_add_f32 v102, v104, v105\n\t"
        "v_add_f32 v103, v106, v107\n\t"
        "s_waitcnt lgkmcnt(2)\n\t"
        "v_mfma_f32_16x16x32_bf16 v[52:55], a[48:51], v[76:79], v[52:55]\n\t"
        "ds_read_b128 v[88:91], v37 offset:31744\n\t"
        "v_add_f32 v128, v108, v109\n\t"
        "v_mfma_f32_16x16x32_bf16 v[56:59], v[76:79], a[112:115], v[56:59]\n\t"
        "v_cvt_pk_bf16_f32 v120, v104, v105\n\t"
        "v_cvt_pk_bf16_f32 v121, v106, v107\n\t"
        "v_cvt_pk_bf16_f32 v122, v108, v109\n\t"
        "v_mfma_f32_16x16x32_bf16 v[60:63], a[176:179], v[76:79], v[60:63]\n\t"
        "v_cvt_pk_bf16_f32 v123, v110, v111\n\t"
        "v_add_f32 v102, v102, v103\n\t"
        "s_waitcnt lgkmcnt(2)\n\t"
        "v_mfma_f32_16x16x32_bf16 v[52:55], a[52:55], v[80:83], v[52:55]\n\t"
        "ds_read_b128 v[76:79], v36 offset:32768\n\t"
        "v_add_f32 v103, v110, v111\n\t"
        "v_mfma_f32_16x16x32_bf16 v[56:59], v[80:83], a[116:119], v[56:59]\n\t"
        "v_mfma_f32_16x16x32_bf16 v[124:127], v[112:115], v[120:123], 0\n\t"
        "v_add_f32 v102, v102, v128\n\t"
        "v_mfma_f32_16x16x32_bf16 v[60:63], a[180:183], v[80:83], v[60:63]\n\t"
        "v_add_f32 v102, v102, v103\n\t"
        "v_mov_b32 v103, v102\n\t"
        "s_waitcnt lgkmcnt(2)\n\t"
        "v_mfma_f32_16x16x32_bf16 v[52:55], a[56:59], v[84:87], v[52:55]\n\t"
        "ds_read_b128 v[80:83], v37 offset:33792\n\t"
        "v_permlane16_swap_b32 v102, v103\n\t"
        "v_add_f32 v102, v102, v103\n\t"
        "v_mfma_f32_16x16x32_bf16 v[56:59], v[84:87], a[120:123], v[56:59]\n\t"
        "v_mov_b32 v103, v102\n\t"
        "v_mfma_f32_16x16x32_bf16 v[60:63], a[184:187], v[84:87], v[60:63]\n\t"
        "s_nop 0\n\t"
        "v_permlane32_swap_b32 v102, v103\n\t"
        "v_add_f32 v102, v102, v103\n\t"
        "v_rcp_f32 v102, v102\n\t"
        "s_waitcnt lgkmcnt(2)\n\t"
        "v_mfma_f32_16x16x32_bf16 v[52:55], a[60:63], v[88:91], v[52:55]\n\t"
        "ds_read_b128 v[84:87], v36 offset:34816\n\t"
        "v_mul_f32 v124, v124, v102\n\t"
        "v_mul_f32 v125, v125, v102\n\t"
        "v_mfma_f32_16x16x32_bf16 v[56:59], v[88:91], a[124:127], v[56:59]\n\t"
        "v_mul_f32 v126, v126, v102\n\t"
        "v_mul_f32 v127, v127, v102\n\t"
        "v_mfma_f32_16x16x32_bf16 v[60:63], a[188:191], v[88:91], v[60:63]\n\t"
        "v_cvt_pk_bf16_f32 v132, v124, v125\n\t"
        "v_cvt_pk_bf16_f32 v133, v126, v127\n\t"
        "s_waitcnt lgkmcnt(2)\n\t"
        "v_mfma_f32_16x16x32_bf16 v[64:67], a[0:3], v[76:79], 0\n\t"
        "ds_read_b128 v[88:91], v37 offset:35840\n\t"
        "v_cvt_pk_bf16_f32 v92, v52, v53\n\t"
        "buffer_load_dwordx4 a[224:227], v2, s[56:59], s7 offen offset:0 sc1\n\t"
        "v_mfma_f32_16x16x32_bf16 v[68:71], v[76:79], a[64:67], 0\n\t"
        "v_cvt_pk_bf16_f32 v93, v54, v55\n\t"
        "buffer_load_dwordx4 a[228:231], v2, s[56:59], s7 offen offset:1024 sc1\n\t"
        "v_mfma_f32_16x16x32_bf16 v[72:75], a[128:131], v[76:79], 0\n\t"
        "v_cvt_pk_bf16_f32 v100, v60, v61\n\t"
        "buffer_load_dwordx4 a[232:235], v2, s[56:59], s7 offen offset:2048 sc1\n\t"
        "s_waitcnt lgkmcnt(2)\n\t"
        "v_mfma_f32_16x16x32_bf16 v[64:67], a[4:7], v[80:83], v[64:67]\n\t"
        "ds_read_b128 v[76:79], v36 offset:36864\n\t"
        "v_cvt_pk_bf16_f32 v101, v62, v63\n\t"
        "v_mfma_f32_16x16x32_bf16 v[68:71], v[80:83], a[68:71], v[68:71]\n\t"
        "buffer_load_dwordx4 a[236:239], v2, s[56:59], s7 offen offset:3072 sc1\n\t"
        "v_cvt_pk_bf16_f32 v94, v56, v57\n\t"
        "v_mfma_f32_16x16x32_bf16 v[72:75], a[132:135], v[80:83], v[72:75]\n\t"
        "s_add_u32 s7, s7, 4096\n\t"
        "v_cvt_pk_bf16_f32 v95, v58, v59\n\t"
        "buffer_load_dwordx4 a[240:243], v2, s[56:59], s7 offen offset:0 sc1\n\t"
        "s_waitcnt lgkmcnt(2)\n\t"
        "v_mfma_f32_16x16x32_bf16 v[64:67], a[8:11], v[84:87], v[64:67]\n\t"
        "ds_read_b128 v[80:83], v37 offset:37888\n\t"
        "v_mfma_f32_16x16x16_bf16 v[112:115], v[96:97], v[100:101], 0\n\t"
        "v_mfma_f32_16x16x32_bf16 v[68:71], v[84:87], a[72:75], v[68:71]\n\t"
        "buffer_load_dwordx4 a[244:247], v2, s[56:59], s7 offen offset:1024 sc1\n\t"
        "v_mfma_f32_16x16x16_bf16 v[116:119], v[92:93], v[100:101], 0\n\t"
        "v_mfma_f32_16x16x32_bf16 v[72:75], a[136:139], v[84:87], v[72:75]\n\t"
        "buffer_load_dwordx4 a[248:251], v2, s[56:59], s7 offen offset:2048 sc1\n\t"
        "buffer_load_dwordx4 a[252:255], v2, s[56:59], s7 offen offset:3072 sc1\n\t"
        "s_waitcnt lgkmcnt(2)\n\t"
        "v_mfma_f32_16x16x32_bf16 v[64:67], a[12:15], v[88:91], v[64:67]\n\t"
        "ds_read_b128 v[84:87], v36 offset:38912\n\t"
        "v_fma_f32 v104, v112, s5, v32\n\t"
        "v_mfma_f32_16x16x32_bf16 v[68:71], v[88:91], a[76:79], v[68:71]\n\t"
        "v_fma_f32 v105, v113, s5, v33\n\t"
        "v_fma_f32 v106, v114, s5, v34\n\t"
        "v_fma_f32 v107, v115, s5, v35\n\t"
        "v_mfma_f32_16x16x32_bf16 v[72:75], a[140:143], v[88:91], v[72:75]\n\t"
        "v_fma_f32 v108, v116, s5, v48\n\t"
        "v_fma_f32 v109, v117, s5, v49\n\t"
        "s_waitcnt lgkmcnt(2)\n\t"
        "v_mfma_f32_16x16x32_bf16 v[64:67], a[16:19], v[76:79], v[64:67]\n\t"
        "ds_read_b128 v[88:91], v37 offset:39936\n\t"
        "v_fma_f32 v110, v118, s5, v50\n\t"
        "v_mfma_f32_16x16x32_bf16 v[68:71], v[76:79], a[80:83], v[68:71]\n\t"
        "v_fma_f32 v111, v119, s5, v51\n\t"
        "v_max3_f32 v102, v104, v105, v106\n\t"
        "v_mfma_f32_16x16x32_bf16 v[72:75], a[144:147], v[76:79], v[72:75]\n\t"
        "v_max3_f32 v103, v107, v108, v109\n\t"
        "v_max3_f32 v102, v110, v111, v102\n\t"
        "v_mov_b32 v112, v98\n\t"
        "s_waitcnt lgkmcnt(2)\n\t"
        "v_mfma_f32_16x16x32_bf16 v[64:67], a[20:23], v[80:83], v[64:67]\n\t"
        "ds_read_b128 v[76:79], v36 offset:40960\n\t"
        "v_max_f32 v102, v102, v103\n\t"
        "v_mfma_f32_16x16x32_bf16 v[68:71], v[80:83], a[84:87], v[68:71]\n\t"
        "v_mov_b32 v103, v102\n\t"
        "v_mov_b32 v113, v99\n\t"
        "v_mfma_f32_16x16x32_bf16 v[72:75], a[148:151], v[80:83], v[72:75]\n\t"
        "v_permlane16_swap_b32 v102, v103\n\t"
        "v_max_f32 v102, v102, v103\n\t"
        "s_waitcnt lgkmcnt(2)\n\t"
        "v_mfma_f32_16x16x32_bf16 v[64:67], a[24:27], v[84:87], v[64:67]\n\t"
        "ds_read_b128 v[80:83], v37 offset:41984\n\t"
        "v_mov_b32 v103, v102\n\t"
        "v_mfma_f32_16x16x32_bf16 v[68:71], v[84:87], a[88:91], v[68:71]\n\t"
        "v_mov_b32 v114, v94\n\t"
        "v_permlane32_swap_b32 v102, v103\n\t"
        "v_max_f32 v102, v102, v103\n\t"
        "v_mfma_f32_16x16x32_bf16 v[72:75], a[152:155], v[84:87], v[72:75]\n\t"
        "v_mov_b32 v115, v95\n\t"
        "v_sub_f32 v104, v104, v102\n\t"
        "s_waitcnt lgkmcnt(2)\n\t"
        "v_mfma_f32_16x16x32_bf16 v[64:67], a[28:31], v[88:91], v[64:67]\n\t"
        "ds_read_b128 v[84:87], v36 offset:43008\n\t"
        "v_sub_f32 v105, v105, v102\n\t"
        "v_mfma_f32_16x16x32_bf16 v[68:71], v[88:91], a[92:95], v[68:71]\n\t"
        "v_sub_f32 v106, v106, v102\n\t"
        "v_sub_f32 v107, v107, v102\n\t"
        "v_mfma_f32_16x16x32_bf16 v[72:75], a[156:159], v[88:91], v[72:75]\n\t"
        "v_sub_f32 v108, v108, v102\n\t"
        "v_sub_f32 v109, v109, v102\n\t"
        "s_waitcnt lgkmcnt(2)\n\t"
        "v_mfma_f32_16x16x32_bf16 v[64:67], a[32:35], v[76:79], v[64:67]\n\t"
        "ds_read_b128 v[88:91], v37 offset:44032\n\t"
        "v_sub_f32 v110, v110, v102\n\t"
        "buffer_load_dwordx4 v[160:163], v2, s[56:59], s11 offen offset:0 sc1\n\t"
        "v_mfma_f32_16x16x32_bf16 v[68:71], v[76:79], a[96:99], v[68:71]\n\t"
        "v_sub_f32 v111, v111, v102\n\t"
        "buffer_load_dwordx4 v[164:167], v2, s[56:59], s11 offen offset:1024 sc1\n\t"
        "v_mfma_f32_16x16x32_bf16 v[72:75], a[160:163], v[76:79], v[72:75]\n\t"
        "v_exp_f32 v104, v104\n\t"
        "buffer_load_dwordx4 v[168:171], v2, s[56:59], s11 offen offset:2048 sc1\n\t"
        "s_waitcnt lgkmcnt(2)\n\t"
        "v_mfma_f32_16x16x32_bf16 v[64:67], a[36:39], v[80:83], v[64:67]\n\t"
        "ds_read_b128 v[76:79], v36 offset:45056\n\t"
        "v_exp_f32 v105, v105\n\t"
        "buffer_load_dwordx4 v[172:175], v2, s[56:59], s11 offen offset:3072 sc1\n\t"
        "v_mfma_f32_16x16x32_bf16 v[68:71], v[80:83], a[100:103], v[68:71]\n\t"
        "v_exp_f32 v106, v106\n\t"
        "s_add_u32 s11, s11, 4096\n\t"
        "v_mfma_f32_16x16x32_bf16 v[72:75], a[164:167], v[80:83], v[72:75]\n\t"
        "v_exp_f32 v107, v107\n\t"
        "buffer_load_dwordx4 v[176:179], v2, s[56:59], s11 offen offset:0 sc1\n\t"
        "s_waitcnt lgkmcnt(2)\n\t"
        "v_mfma_f32_16x16x32_bf16 v[64:67], a[40:43], v[84:87], v[64:67]\n\t"
        "ds_read_b128 v[80:83], v37 offset:46080\n\t"
        "v_exp_f32 v108, v108\n\t"
        "v_mfma_f32_16x16x32_bf16 v[68:71], v[84:87], a[104:107], v[68:71]\n\t"
        "buffer_load_dwordx4 v[180:183], v2, s[56:59], s11 offen offset:1024 sc1\n\t"
        "v_exp_f32 v109, v109\n\t"
        "buffer_load_dwordx4 v[184:187], v2, s[56:59], s11 offen offset:2048 sc1\n\t"
        "v_mfma_f32_16x16x32_bf16 v[72:75], a[168:171], v[84:87], v[72:75]\n\t"
        "v_exp_f32 v110, v110\n\t"
        "buffer_load_dwordx4 v[188:191], v2, s[56:59], s11 offen offset:3072 sc1\n\t"
        "s_waitcnt lgkmcnt(2)\n\t"
        "v_mfma_f32_16x16x32_bf16 v[64:67], a[44:47], v[88:91], v[64:67]\n\t"
        "ds_read_b128 v[84:87], v36 offset:47104\n\t"
        "v_exp_f32 v111, v111\n\t"
        "v_mfma_f32_16x16x32_bf16 v[68:71], v[88:91], a[108:111], v[68:71]\n\t"
        "s_add_u32 s11, s11, 4096\n\t"
        "v_add_f32 v102, v104, v105\n\t"
        "buffer_load_dwordx4 v[192:195], v2, s[56:59], s11 offen offset:0 sc1\n\t"
        "v_mfma_f32_16x16x32_bf16 v[72:75], a[172:175], v[88:91], v[72:75]\n\t"
        "v_add_f32 v103, v106, v107\n\t"
        "buffer_load_dwordx4 v[196:199], v2, s[56:59], s11 offen offset:1024 sc1\n\t"
        "s_waitcnt lgkmcnt(2)\n\t"
        "v_mfma_f32_16x16x32_bf16 v[64:67], a[48:51], v[76:79], v[64:67]\n\t"
        "ds_read_b128 v[88:91], v37 offset:48128\n\t"
        "v_add_f32 v128, v108, v109\n\t"
        "v_mfma_f32_16x16x32_bf16 v[68:71], v[76:79], a[112:115], v[68:71]\n\t"
        "v_cvt_pk_bf16_f32 v120, v104, v105\n\t"
        "v_cvt_pk_bf16_f32 v121, v106, v107\n\t"
        "v_cvt_pk_bf16_f32 v122, v108, v109\n\t"
        "v_mfma_f32_16x16x32_bf16 v[72:75], a[176:179], v[76:79], v[72:75]\n\t"
        "v_cvt_pk_bf16_f32 v123, v110, v111\n\t"
        "v_add_f32 v102, v102, v103\n\t"
        "s_waitcnt lgkmcnt(2)\n\t"
        "v_mfma_f32_16x16x32_bf16 v[64:67], a[52:55], v[80:83], v[64:67]\n\t"
        "ds_read_b128 v[76:79], v36 offset:49152\n\t"
        "v_add_f32 v103, v110, v111\n\t"
        "v_mfma_f32_16x16x32_bf16 v[68:71], v[80:83], a[116:119], v[68:71]\n\t"
        "v_mfma_f32_16x16x32_bf16 v[124:127], v[112:115], v[120:123], 0\n\t"
        "v_add_f32 v102, v102, v128\n\t"
        "v_mfma_f32_16x16x32_bf16 v[72:75], a[180:183], v[80:83], v[72:75]\n\t"
        "v_add_f32 v102, v102, v103\n\t"
        "v_mov_b32 v103, v102\n\t"
        "s_waitcnt lgkmcnt(2)\n\t"
        "v_mfma_f32_16x16x32_bf16 v[64:67], a[56:59], v[84:87], v[64:67]\n\t"
        "ds_read_b128 v[80:83], v37 offset:50176\n\t"
        "v_permlane16_swap_b32 v102, v103\n\t"
        "v_add_f32 v102, v102, v103\n\t"
        "v_mfma_f32_16x16x32_bf16 v[68:71], v[84:87], a[120:123], v[68:71]\n\t"
        "v_mov_b32 v103, v102\n\t"
        "v_mfma_f32_16x16x32_bf16 v[72:75], a[184:187], v[84:87], v[72:75]\n\t"
        "s_nop 0\n\t"
        "v_permlane32_swap_b32 v102, v103\n\t"
        "v_add_f32 v102, v102, v103\n\t"
        "v_rcp_f32 v102, v102\n\t"
        "s_waitcnt lgkmcnt(2)\n\t"
        "v_mfma_f32_16x16x32_bf16 v[64:67], a[60:63], v[88:91], v[64:67]\n\t"
        "ds_read_b128 v[84:87], v36 offset:51200\n\t"
        "v_mul_f32 v124, v124, v102\n\t"
        "v_mul_f32 v125, v125, v102\n\t"
        "v_mfma_f32_16x16x32_bf16 v[68:71], v[88:91], a[124:127], v[68:71]\n\t"
        "v_mul_f32 v126, v126, v102\n\t"
        "v_mul_f32 v127, v127, v102\n\t"
        "v_mfma_f32_16x16x32_bf16 v[72:75], a[188:191], v[88:91], v[72:75]\n\t"
        "v_cvt_pk_bf16_f32 v134, v124, v125\n\t"
        "v_cvt_pk_bf16_f32 v135, v126, v127\n\t"
        "s_waitcnt lgkmcnt(2)\n\t"
        "v_mfma_f32_16x16x32_bf16 v[52:55], a[0:3], v[76:79], 0\n\t"
        "ds_read_b128 v[88:91], v37 offset:52224\n\t"
        "v_cvt_pk_bf16_f32 v96, v64, v65\n\t"
        "buffer_load_dwordx4 v[200:203], v2, s[56:59], s11 offen offset:2048 sc1\n\t"
        "v_mfma_f32_16x16x32_bf16 v[56:59], v[76:79], a[64:67], 0\n\t"
        "v_cvt_pk_bf16_f32 v97, v66, v67\n\t"
        "buffer_load_dwordx4 v[204:207], v2, s[56:59], s11 offen offset:3072 sc1\n\t"
        "v_mfma_f32_16x16x32_bf16 v[60:63], a[128:131], v[76:79], 0\n\t"
        "v_cvt_pk_bf16_f32 v100, v72, v73\n\t"
        "s_add_u32 s11, s11, 4096\n\t"
        "s_waitcnt lgkmcnt(2)\n\t"
        "v_mfma_f32_16x16x32_bf16 v[52:55], a[4:7], v[80:83], v[52:55]\n\t"
        "ds_read_b128 v[76:79], v36 offset:53248\n\t"
        "v_cvt_pk_bf16_f32 v101, v74, v75\n\t"
        "v_mfma_f32_16x16x32_bf16 v[56:59], v[80:83], a[68:71], v[56:59]\n\t"
        "buffer_load_dwordx4 v[208:211], v2, s[56:59], s11 offen offset:0 sc1\n\t"
        "v_cvt_pk_bf16_f32 v98, v68, v69\n\t"
        "v_mfma_f32_16x16x32_bf16 v[60:63], a[132:135], v[80:83], v[60:63]\n\t"
        "buffer_load_dwordx4 v[212:215], v2, s[56:59], s11 offen offset:1024 sc1\n\t"
        "v_cvt_pk_bf16_f32 v99, v70, v71\n\t"
        "buffer_load_dwordx4 v[216:219], v2, s[56:59], s11 offen offset:2048 sc1\n\t"
        "s_waitcnt lgkmcnt(2)\n\t"
        "v_mfma_f32_16x16x32_bf16 v[52:55], a[8:11], v[84:87], v[52:55]\n\t"
        "ds_read_b128 v[80:83], v37 offset:54272\n\t"
        "v_mfma_f32_16x16x16_bf16 v[112:115], v[92:93], v[100:101], 0\n\t"
        "v_mfma_f32_16x16x32_bf16 v[56:59], v[84:87], a[72:75], v[56:59]\n\t"
        "buffer_load_dwordx4 v[220:223], v2, s[56:59], s11 offen offset:3072 sc1\n\t"
        "v_mfma_f32_16x16x16_bf16 v[116:119], v[96:97], v[100:101], 0\n\t"
        "v_mfma_f32_16x16x32_bf16 v[60:63], a[136:139], v[84:87], v[60:63]\n\t"
        "buffer_load_dwordx4 v[224:227], v2, s[56:59], s12 offen offset:0 sc1\n\t"
        "buffer_load_dwordx4 v[228:231], v2, s[56:59], s12 offen offset:1024 sc1\n\t"
        "s_waitcnt lgkmcnt(2)\n\t"
        "v_mfma_f32_16x16x32_bf16 v[52:55], a[12:15], v[88:91], v[52:55]\n\t"
        "ds_read_b128 v[84:87], v36 offset:55296\n\t"
        "v_fma_f32 v104, v112, s5, v32\n\t"
        "v_mfma_f32_16x16x32_bf16 v[56:59], v[88:91], a[76:79], v[56:59]\n\t"
        "v_fma_f32 v105, v113, s5, v33\n\t"
        "v_fma_f32 v106, v114, s5, v34\n\t"
        "v_fma_f32 v107, v115, s5, v35\n\t"
        "v_mfma_f32_16x16x32_bf16 v[60:63], a[140:143], v[88:91], v[60:63]\n\t"
        "v_fma_f32 v108, v116, s5, v48\n\t"
        "v_fma_f32 v109, v117, s5, v49\n\t"
        "s_waitcnt lgkmcnt(2)\n\t"
        "v_mfma_f32_16x16x32_bf16 v[52:55], a[16:19], v[76:79], v[52:55]\n\t"
        "ds_read_b128 v[88:91], v37 offset:56320\n\t"
        "v_fma_f32 v110, v118, s5, v50\n\t"
        "v_mfma_f32_16x16x32_bf16 v[56:59], v[76:79], a[80:83], v[56:59]\n\t"
        "v_fma_f32 v111, v119, s5, v51\n\t"
        "v_max3_f32 v102, v104, v105, v106\n\t"
        "v_mfma_f32_16x16x32_bf16 v[60:63], a[144:147], v[76:79], v[60:63]\n\t"
        "v_max3_f32 v103, v107, v108, v109\n\t"
        "v_max3_f32 v102, v110, v111, v102\n\t"
        "v_mov_b32 v112, v94\n\t"
        "s_waitcnt lgkmcnt(2)\n\t"
        "v_mfma_f32_16x16x32_bf16 v[52:55], a[20:23], v[80:83], v[52:55]\n\t"
        "ds_read_b128 v[76:79], v36 offset:57344\n\t"
        "v_max_f32 v102, v102, v103\n\t"
        "v_mfma_f32_16x16x32_bf16 v[56:59], v[80:83], a[84:87], v[56:59]\n\t"
        "v_mov_b32 v103, v102\n\t"
        "v_mov_b32 v113, v95\n\t"
        "v_mfma_f32_16x16x32_bf16 v[60:63], a[148:151], v[80:83], v[60:63]\n\t"
        "v_permlane16_swap_b32 v102, v103\n\t"
        "v_max_f32 v102, v102, v103\n\t"
        "s_waitcnt lgkmcnt(2)\n\t"
        "v_mfma_f32_16x16x32_bf16 v[52:55], a[24:27], v[84:87], v[52:55]\n\t"
        "ds_read_b128 v[80:83], v37 offset:58368\n\t"
        "v_mov_b32 v103, v102\n\t"
        "v_mfma_f32_16x16x32_bf16 v[56:59], v[84:87], a[88:91], v[56:59]\n\t"
        "v_mov_b32 v114, v98\n\t"
        "v_permlane32_swap_b32 v102, v103\n\t"
        "v_max_f32 v102, v102, v103\n\t"
        "v_mfma_f32_16x16x32_bf16 v[60:63], a[152:155], v[84:87], v[60:63]\n\t"
        "v_mov_b32 v115, v99\n\t"
        "v_sub_f32 v104, v104, v102\n\t"
        "s_waitcnt lgkmcnt(2)\n\t"
        "v_mfma_f32_16x16x32_bf16 v[52:55], a[28:31], v[88:91], v[52:55]\n\t"
        "ds_read_b128 v[84:87], v36 offset:59392\n\t"
        "v_sub_f32 v105, v105, v102\n\t"
        "v_mfma_f32_16x16x32_bf16 v[56:59], v[88:91], a[92:95], v[56:59]\n\t"
        "v_sub_f32 v106, v106, v102\n\t"
        "v_sub_f32 v107, v107, v102\n\t"
        "v_mfma_f32_16x16x32_bf16 v[60:63], a[156:159], v[88:91], v[60:63]\n\t"
        "v_sub_f32 v108, v108, v102\n\t"
        "v_sub_f32 v109, v109, v102\n\t"
        "s_waitcnt lgkmcnt(2)\n\t"
        "v_mfma_f32_16x16x32_bf16 v[52:55], a[32:35], v[76:79], v[52:55]\n\t"
        "ds_read_b128 v[88:91], v37 offset:60416\n\t"
        "v_sub_f32 v110, v110, v102\n\t"
        "v_mfma_f32_16x16x32_bf16 v[56:59], v[76:79], a[96:99], v[56:59]\n\t"
        "buffer_load_dwordx4 v[232:235], v2, s[56:59], s12 offen offset:2048 sc1\n\t"
        "v_sub_f32 v111, v111, v102\n\t"
        "v_mfma_f32_16x16x32_bf16 v[60:63], a[160:163], v[76:79], v[60:63]\n\t"
        "buffer_load_dwordx4 v[236:239], v2, s[56:59], s12 offen offset:3072 sc1\n\t"
        "v_exp_f32 v104, v104\n\t"
        "s_waitcnt lgkmcnt(2)\n\t"
        "v_mfma_f32_16x16x32_bf16 v[52:55], a[36:39], v[80:83], v[52:55]\n\t"
        "ds_read_b128 v[76:79], v36 offset:61440\n\t"
        "s_add_u32 s12, s12, 4096\n\t"
        "v_mfma_f32_16x16x32_bf16 v[56:59], v[80:83], a[100:103], v[56:59]\n\t"
        "v_exp_f32 v105, v105\n\t"
        "buffer_load_dwordx4 v[240:243], v2, s[56:59], s12 offen offset:0 sc1\n\t"
        "v_mfma_f32_16x16x32_bf16 v[60:63], a[164:167], v[80:83], v[60:63]\n\t"
        "v_exp_f32 v106, v106\n\t"
        "buffer_load_dwordx4 v[244:247], v2, s[56:59], s12 offen offset:1024 sc1\n\t"
        "s_waitcnt lgkmcnt(2)\n\t"
        "v_mfma_f32_16x16x32_bf16 v[52:55], a[40:43], v[84:87], v[52:55]\n\t"
        "ds_read_b128 v[80:83], v37 offset:62464\n\t"
        "v_exp_f32 v107, v107\n\t"
        "v_mfma_f32_16x16x32_bf16 v[56:59], v[84:87], a[104:107], v[56:59]\n\t"
        "buffer_load_dwordx4 v[248:251], v2, s[56:59], s12 offen offset:2048 sc1\n\t"
        "v_exp_f32 v108, v108\n\t"
        "v_mfma_f32_16x16x32_bf16 v[60:63], a[168:171], v[84:87], v[60:63]\n\t"
        "buffer_load_dwordx4 v[252:255], v2, s[56:59], s12 offen offset:3072 sc1\n\t"
        "v_exp_f32 v109, v109\n\t"
        "s_waitcnt lgkmcnt(2)\n\t"
        "v_mfma_f32_16x16x32_bf16 v[52:55], a[44:47], v[88:91], v[52:55]\n\t"
        "ds_read_b128 v[84:87], v36 offset:63488\n\t"
        "v_exp_f32 v110, v110\n\t"
        "v_mfma_f32_16x16x32_bf16 v[56:59], v[88:91], a[108:111], v[56:59]\n\t"
        "v_exp_f32 v111, v111\n\t"
        "v_add_f32 v102, v104, v105\n\t"
        "v_mfma_f32_16x16x32_bf16 v[60:63], a[172:175], v[88:91], v[60:63]\n\t"
        "v_add_f32 v103, v106, v107\n\t"
        "v_add_f32 v128, v108, v109\n\t"
        "s_waitcnt lgkmcnt(2)\n\t"
        "v_mfma_f32_16x16x32_bf16 v[52:55], a[48:51], v[76:79], v[52:55]\n\t"
        "ds_read_b128 v[88:91], v37 offset:64512\n\t"
        "v_cvt_pk_bf16_f32 v120, v104, v105\n\t"
        "v_mfma_f32_16x16x32_bf16 v[56:59], v[76:79], a[112:115], v[56:59]\n\t"
        "v_cvt_pk_bf16_f32 v121, v106, v107\n\t"
        "v_cvt_pk_bf16_f32 v122, v108, v109\n\t"
        "v_mfma_f32_16x16x32_bf16 v[60:63], a[176:179], v[76:79], v[60:63]\n\t"
        "v_cvt_pk_bf16_f32 v123, v110, v111\n\t"
        "v_add_f32 v102, v102, v103\n\t"
        "s_waitcnt lgkmcnt(2)\n\t"
        "v_mfma_f32_16x16x32_bf16 v[52:55], a[52:55], v[80:83], v[52:55]\n\t"
        "v_add_f32 v103, v110, v111\n\t"
        "v_mfma_f32_16x16x32_bf16 v[124:127], v[112:115], v[120:123], 0\n\t"
        "v_mfma_f32_16x16x32_bf16 v[56:59], v[80:83], a[116:119], v[56:59]\n\t"
        "v_add_f32 v102, v102, v128\n\t"
        "v_add_f32 v102, v102, v103\n\t"
        "v_mfma_f32_16x16x32_bf16 v[60:63], a[180:183], v[80:83], v[60:63]\n\t"
        "v_mov_b32 v103, v102\n\t"
        "s_waitcnt lgkmcnt(1)\n\t"
        "v_mfma_f32_16x16x32_bf16 v[52:55], a[56:59], v[84:87], v[52:55]\n\t"
        "v_permlane16_swap_b32 v102, v103\n\t"
        "v_add_f32 v102, v102, v103\n\t"
        "v_mov_b32 v103, v102\n\t"
        "v_mfma_f32_16x16x32_bf16 v[56:59], v[84:87], a[120:123], v[56:59]\n\t"
        "v_mfma_f32_16x16x32_bf16 v[60:63], a[184:187], v[84:87], v[60:63]\n\t"
        "v_permlane32_swap_b32 v102, v103\n\t"
        "v_add_f32 v102, v102, v103\n\t"
        "v_rcp_f32 v102, v102\n\t"
        "s_waitcnt lgkmcnt(0)\n\t"
        "v_mfma_f32_16x16x32_bf16 v[52:55], a[60:63], v[88:91], v[52:55]\n\t"
        "v_mul_f32 v124, v124, v102\n\t"
        "v_mul_f32 v125, v125, v102\n\t"
        "v_mul_f32 v126, v126, v102\n\t"
        "v_mfma_f32_16x16x32_bf16 v[56:59], v[88:91], a[124:127], v[56:59]\n\t"
        "v_mul_f32 v127, v127, v102\n\t"
        "v_cvt_pk_bf16_f32 v136, v124, v125\n\t"
        "v_mfma_f32_16x16x32_bf16 v[60:63], a[188:191], v[88:91], v[60:63]\n\t"
        "v_cvt_pk_bf16_f32 v137, v126, v127\n\t"
        "v_and_b32 v6, 1, v3\n\t"
        "v_lshlrev_b32 v6, 14, v6\n\t"
        "v_lshrrev_b32 v7, 1, v3\n\t"
        "v_lshl_add_u32 v7, v7, 4, v4\n\t"
        "v_lshl_add_u32 v10, v7, 4, v6\n\t"
        "s_lshl_b32 s15, s29, 16\n\t"
        "s_lshl_b32 s33, s27, 11\n\t"
        "s_add_u32 s15, s15, s33\n\t"
        "s_lshr_b32 s33, s24, 1\n\t"
        "s_lshl_b32 s33, s33, 10\n\t"
        "s_add_u32 s15, s15, s33\n\t"
        "s_and_b32 s33, s24, 1\n\t"
        "s_lshl_b32 s33, s33, 9\n\t"
        "s_add_u32 s15, s15, s33\n\t"
        "s_add_u32 s15, s15, 5242880\n\t"
        "s_add_u32 s16, s15, 32768\n\t"
        "v_permlane16_swap_b32 v132, v134\n\t"
        "v_permlane16_swap_b32 v133, v135\n\t"
        "buffer_store_dwordx4 v[132:135], v10, s[56:59], s15 offen sc1\n\t"
        "v_cvt_pk_bf16_f32 v92, v52, v53\n\t"
        "v_cvt_pk_bf16_f32 v93, v54, v55\n\t"
        "v_cvt_pk_bf16_f32 v100, v60, v61\n\t"
        "v_cvt_pk_bf16_f32 v101, v62, v63\n\t"
        "v_cvt_pk_bf16_f32 v94, v56, v57\n\t"
        "v_cvt_pk_bf16_f32 v95, v58, v59\n\t"
        "v_mfma_f32_16x16x16_bf16 v[112:115], v[96:97], v[100:101], 0\n\t"
        "v_mfma_f32_16x16x16_bf16 v[116:119], v[92:93], v[100:101], 0\n\t"
        "s_nop 6\n\t"
        "v_fma_f32 v104, v112, s5, v32\n\t"
        "v_fma_f32 v105, v113, s5, v33\n\t"
        "v_fma_f32 v106, v114, s5, v34\n\t"
        "v_fma_f32 v107, v115, s5, v35\n\t"
        "v_fma_f32 v108, v116, s5, v48\n\t"
        "v_fma_f32 v109, v117, s5, v49\n\t"
        "v_fma_f32 v110, v118, s5, v50\n\t"
        "v_fma_f32 v111, v119, s5, v51\n\t"
        "v_max3_f32 v102, v104, v105, v106\n\t"
        "v_max3_f32 v103, v107, v108, v109\n\t"
        "v_max3_f32 v102, v110, v111, v102\n\t"
        "v_mov_b32 v112, v98\n\t"
        "v_max_f32 v102, v102, v103\n\t"
        "v_mov_b32 v103, v102\n\t"
        "v_mov_b32 v113, v99\n\t"
        "s_nop 0\n\t"
        "v_permlane16_swap_b32 v102, v103\n\t"
        "v_max_f32 v102, v102, v103\n\t"
        "v_mov_b32 v103, v102\n\t"
        "v_mov_b32 v114, v94\n\t"
        "s_nop 0\n\t"
        "v_permlane32_swap_b32 v102, v103\n\t"
        "v_max_f32 v102, v102, v103\n\t"
        "v_mov_b32 v115, v95\n\t"
        "v_sub_f32 v104, v104, v102\n\t"
        "v_sub_f32 v105, v105, v102\n\t"
        "v_sub_f32 v106, v106, v102\n\t"
        "v_sub_f32 v107, v107, v102\n\t"
        "v_sub_f32 v108, v108, v102\n\t"
        "v_sub_f32 v109, v109, v102\n\t"
        "v_sub_f32 v110, v110, v102\n\t"
        "v_sub_f32 v111, v111, v102\n\t"
        "v_exp_f32 v104, v104\n\t"
        "v_exp_f32 v105, v105\n\t"
        "v_exp_f32 v106, v106\n\t"
        "v_exp_f32 v107, v107\n\t"
        "v_exp_f32 v108, v108\n\t"
        "v_exp_f32 v109, v109\n\t"
        "v_exp_f32 v110, v110\n\t"
        "v_exp_f32 v111, v111\n\t"
        "v_add_f32 v102, v104, v105\n\t"
        "v_add_f32 v103, v106, v107\n\t"
        "v_add_f32 v128, v108, v109\n\t"
        "v_cvt_pk_bf16_f32 v120, v104, v105\n\t"
        "v_cvt_pk_bf16_f32 v121, v106, v107\n\t"
        "v_cvt_pk_bf16_f32 v122, v108, v109\n\t"
        "v_cvt_pk_bf16_f32 v123, v110, v111\n\t"
        "v_add_f32 v102, v102, v103\n\t"
        "v_add_f32 v103, v110, v111\n\t"
        "v_mfma_f32_16x16x32_bf16 v[124:127], v[112:115], v[120:123], 0\n\t"
        "v_add_f32 v102, v102, v128\n\t"
        "v_add_f32 v102, v102, v103\n\t"
        "v_mov_b32 v103, v102\n\t"
        "s_nop 1\n\t"
        "v_permlane16_swap_b32 v102, v103\n\t"
        "v_add_f32 v102, v102, v103\n\t"
        "v_mov_b32 v103, v102\n\t"
        "s_nop 1\n\t"
        "v_permlane32_swap_b32 v102, v103\n\t"
        "v_add_f32 v102, v102, v103\n\t"
        "v_rcp_f32 v102, v102\n\t"
        "s_nop 0\n\t"
        "v_mul_f32 v124, v124, v102\n\t"
        "v_mul_f32 v125, v125, v102\n\t"
        "v_mul_f32 v126, v126, v102\n\t"
        "v_mul_f32 v127, v127, v102\n\t"
        "v_cvt_pk_bf16_f32 v138, v124, v125\n\t"
        "v_cvt_pk_bf16_f32 v139, v126, v127\n\t"
        "s_nop 0\n\t"
        "v_permlane16_swap_b32 v136, v138\n\t"
        "v_permlane16_swap_b32 v137, v139\n\t"
        "buffer_store_dwordx4 v[136:139], v10, s[56:59], s16 offen sc1\n\t"
        "s_lshl_b32 s4, s29, 7\n\t"
        "s_add_u32 s4, s4, 4096\n\t"
        "s_lshl_b32 s31, s27, 3\n\t"
        "s_add_u32 s5, s4, s31\n\t"
        "s_add_u32 s12, s12, 4096\n\t"
        "buffer_load_dwordx4 a[0:3], v2, s[56:59], s12 offen offset:0 sc1\n\t"
        "buffer_load_dwordx4 a[4:7], v2, s[56:59], s12 offen offset:1024 sc1\n\t"
        "buffer_load_dwordx4 a[8:11], v2, s[56:59], s12 offen offset:2048 sc1\n\t"
        "buffer_load_dwordx4 a[12:15], v2, s[56:59], s12 offen offset:3072 sc1\n\t"
        "s_add_u32 s12, s12, 4096\n\t"
        "buffer_load_dwordx4 a[16:19], v2, s[56:59], s12 offen offset:0 sc1\n\t"
        "buffer_load_dwordx4 a[20:23], v2, s[56:59], s12 offen offset:1024 sc1\n\t"
        "buffer_load_dwordx4 a[24:27], v2, s[56:59], s12 offen offset:2048 sc1\n\t"
        "buffer_load_dwordx4 a[28:31], v2, s[56:59], s12 offen offset:3072 sc1\n\t"
        "buffer_load_dwordx4 a[32:35], v2, s[56:59], s13 offen offset:0 sc1\n\t"
        "buffer_load_dwordx4 a[36:39], v2, s[56:59], s13 offen offset:1024 sc1\n\t"
        "buffer_load_dwordx4 a[40:43], v2, s[56:59], s13 offen offset:2048 sc1\n\t"
        "buffer_load_dwordx4 a[44:47], v2, s[56:59], s13 offen offset:3072 sc1\n\t"
        "s_add_u32 s13, s13, 4096\n\t"
        "buffer_load_dwordx4 a[48:51], v2, s[56:59], s13 offen offset:0 sc1\n\t"
        "buffer_load_dwordx4 a[52:55], v2, s[56:59], s13 offen offset:1024 sc1\n\t"
        "buffer_load_dwordx4 a[56:59], v2, s[56:59], s13 offen offset:2048 sc1\n\t"
        "buffer_load_dwordx4 a[60:63], v2, s[56:59], s13 offen offset:3072 sc1\n\t"
        "s_waitcnt vmcnt(16)\n\t"
        "s_barrier\n\t"
        "s_cmp_lg_u32 s24, 0\n\t"
        "s_cbranch_scc1 .Lpub11\n\t"
        "s_add_u32 s31, s5, 0\n\t"
        "v_mov_b32 v6, 0\n\t"
        "s_mov_b64 exec, 1\n\t"
        "buffer_store_dwordx2 v[14:15], v6, s[56:59], s31 offen sc1\n\t"
        "s_mov_b64 exec, -1\n\t"
        ".Lpub11:\n\t"
        "s_mul_i32 s6, s25, 49152\n\t"
        "s_lshl_b32 s31, s24, 11\n\t"
        "s_add_u32 s6, s6, s31\n\t"
        "s_add_u32 s6, s6, 266240\n\t"
        "s_add_u32 s33, s6, 0\n\t"
        "buffer_load_dwordx4 v[16:19], v2, s[56:59], s33 offen offset:0 sc1\n\t"
        "buffer_load_dwordx4 v[20:23], v2, s[56:59], s33 offen offset:1024 sc1\n\t"
        "s_add_u32 s33, s6, 12288\n\t"
        "buffer_load_dwordx4 v[24:27], v2, s[56:59], s33 offen offset:0 sc1\n\t"
        "buffer_load_dwordx4 v[28:31], v2, s[56:59], s33 offen offset:1024 sc1\n\t"
        "s_add_u32 s33, s6, 24576\n\t"
        "buffer_load_dwordx4 v[32:35], v2, s[56:59], s33 offen offset:0 sc1\n\t"
        "buffer_load_dwordx4 v[36:39], v2, s[56:59], s33 offen offset:1024 sc1\n\t"
        "s_add_u32 s33, s6, 36864\n\t"
        "buffer_load_dwordx4 v[40:43], v2, s[56:59], s33 offen offset:0 sc1\n\t"
        "buffer_load_dwordx4 v[44:47], v2, s[56:59], s33 offen offset:1024 sc1\n\t"
        "v_mov_b32 v8, 0\n\t"
        "v_mov_b32 v9, 0\n\t"
        "v_lshrrev_b32 v6, 1, v2\n\t"
        "v_add_u32 v6, 153600, v6\n\t"
        "ds_write_b64 v6, v[8:9]\n\t"
        "ds_write_b64 v6, v[8:9] offset:512\n\t"
        "s_add_u32 s31, s24, 1\n\t"
        "s_and_b32 s31, s31, 3\n\t"
        "s_mul_i32 s19, s31, 12288\n\t"
        "s_add_u32 s19, s19, 8192\n\t"
        "s_lshl_b32 s33, s29, 2\n\t"
        "s_add_u32 s15, s31, s33\n\t"
        "s_add_u32 s31, s24, 2\n\t"
        "s_and_b32 s31, s31, 3\n\t"
        "s_mul_i32 s20, s31, 12288\n\t"
        "s_add_u32 s20, s20, 4096\n\t"
        "s_lshl_b32 s33, s29, 2\n\t"
        "s_add_u32 s16, s31, s33\n\t"
        "s_add_u32 s31, s24, 3\n\t"
        "s_and_b32 s31, s31, 3\n\t"
        "s_mul_i32 s21, s31, 12288\n\t"
        "s_add_u32 s21, s21, 0\n\t"
        "s_lshl_b32 s33, s29, 2\n\t"
        "s_add_u32 s17, s31, s33\n\t"
        "s_add_u32 s31, s24, 4\n\t"
        "s_and_b32 s31, s31, 3\n\t"
        "s_lshl_b32 s33, s29, 2\n\t"
        "s_add_u32 s18, s31, s33\n\t"
        "v_add_u32 v10, s19, v2\n\t"
        "v_add_u32 v11, s20, v2\n\t"
        "v_add_u32 v12, s21, v2\n\t"
        "s_mul_i32 s31, s24, 12288\n\t"
        "s_add_u32 s31, s31, 0\n\t"
        "v_add_u32 v13, s31, v2\n\t"
        "s_lshl_b32 s19, s15, 14\n\t"
        "s_lshl_b32 s31, s24, 12\n\t"
        "s_add_u32 s19, s19, s31\n\t"
        "s_add_u32 s19, s19, 5242880\n\t"
        "s_lshl_b32 s20, s16, 14\n\t"
        "s_lshl_b32 s31, s24, 12\n\t"
        "s_add_u32 s20, s20, s31\n\t"
        "s_add_u32 s20, s20, 5242880\n\t"
        "s_lshl_b32 s21, s17, 14\n\t"
        "s_lshl_b32 s31, s24, 12\n\t"
        "s_add_u32 s21, s21, s31\n\t"
        "s_add_u32 s21, s21, 5242880\n\t"
        "s_lshl_b32 s22, s18, 14\n\t"
        "s_lshl_b32 s31, s24, 12\n\t"
        "s_add_u32 s22, s22, s31\n\t"
        "s_add_u32 s22, s22, 5242880\n\t"
        "s_cmp_lg_u32 s24, 0\n\t"
        "s_cbranch_scc1 .Lwf12_done\n\t"
        "v_and_b32 v6, 7, v1\n\t"
        "v_lshlrev_b32 v6, 3, v6\n\t"
        "s_add_u32 s33, s4, 0\n\t"
        "s_mov_b32 s31, 65536\n\t"
        ".Lwf12_spin:\n\t"
        "buffer_load_dwordx2 v[8:9], v6, s[56:59], s33 offen sc1\n\t"
        "s_waitcnt vmcnt(0)\n\t"
        "v_cmp_ne_u64 vcc, s[34:35], v[8:9]\n\t"
        "s_nop 0\n\t"
        "s_cbranch_vccz .Lwf12_done\n\t"
        "s_sub_u32 s31, s31, 1\n\t"
        "s_cmp_lg_u32 s31, 0\n\t"
        "s_cbranch_scc1 .Lwf12_spin\n\t"
        ".Lwf12_done:\n\t"
        "s_barrier\n\t"
        "buffer_load_dwordx4 a[96:99], v2, s[56:59], s19 offen offset:0 sc1\n\t"
        "buffer_load_dwordx4 a[100:103], v2, s[56:59], s19 offen offset:1024 sc1\n\t"
        "buffer_load_dwordx4 a[104:107], v2, s[56:59], s19 offen offset:2048 sc1\n\t"
        "buffer_load_dwordx4 a[108:111], v2, s[56:59], s19 offen offset:3072 sc1\n\t"
        "buffer_load_dwordx4 a[112:115], v2, s[56:59], s20 offen offset:0 sc1\n\t"
        "buffer_load_dwordx4 a[116:119], v2, s[56:59], s20 offen offset:1024 sc1\n\t"
        "buffer_load_dwordx4 a[120:123], v2, s[56:59], s20 offen offset:2048 sc1\n\t"
        "buffer_load_dwordx4 a[124:127], v2, s[56:59], s20 offen offset:3072 sc1\n\t"
        "buffer_load_dwordx4 a[128:131], v2, s[56:59], s21 offen offset:0 sc1\n\t"
        "buffer_load_dwordx4 a[132:135], v2, s[56:59], s21 offen offset:1024 sc1\n\t"
        "buffer_load_dwordx4 a[136:139], v2, s[56:59], s21 offen offset:2048 sc1\n\t"
        "buffer_load_dwordx4 a[140:143], v2, s[56:59], s21 offen offset:3072 sc1\n\t"
        "buffer_load_dwordx4 a[144:147], v2, s[56:59], s22 offen offset:0 sc1\n\t"
        "buffer_load_dwordx4 a[148:151], v2, s[56:59], s22 offen offset:1024 sc1\n\t"
        "buffer_load_dwordx4 a[152:155], v2, s[56:59], s22 offen offset:2048 sc1\n\t"
        "buffer_load_dwordx4 a[156:159], v2, s[56:59], s22 offen offset:3072 sc1\n\t"
        "s_waitcnt vmcnt(23)\n\t"
        "v_xor_b32 v6, s34, v17\n\t"
        "v_bitop3_b32 v6, v19, s34, v6 bitop3:0xbe\n\t"
        "s_waitcnt vmcnt(22)\n\t"
        "v_bitop3_b32 v6, v21, s34, v6 bitop3:0xbe\n\t"
        "v_bitop3_b32 v6, v23, s34, v6 bitop3:0xbe\n\t"
        "s_waitcnt vmcnt(21)\n\t"
        "v_bitop3_b32 v6, v25, s34, v6 bitop3:0xbe\n\t"
        "v_bitop3_b32 v6, v27, s34, v6 bitop3:0xbe\n\t"
        "s_waitcnt vmcnt(20)\n\t"
        "v_bitop3_b32 v6, v29, s34, v6 bitop3:0xbe\n\t"
        "v_bitop3_b32 v6, v31, s34, v6 bitop3:0xbe\n\t"
        "s_waitcnt vmcnt(19)\n\t"
        "v_bitop3_b32 v6, v33, s34, v6 bitop3:0xbe\n\t"
        "v_bitop3_b32 v6, v35, s34, v6 bitop3:0xbe\n\t"
        "s_waitcnt vmcnt(18)\n\t"
        "v_bitop3_b32 v6, v37, s34, v6 bitop3:0xbe\n\t"
        "v_bitop3_b32 v6, v39, s34, v6 bitop3:0xbe\n\t"
        "s_waitcnt vmcnt(17)\n\t"
        "v_bitop3_b32 v6, v41, s34, v6 bitop3:0xbe\n\t"
        "v_bitop3_b32 v6, v43, s34, v6 bitop3:0xbe\n\t"
        "s_waitcnt vmcnt(16)\n\t"
        "v_bitop3_b32 v6, v45, s34, v6 bitop3:0xbe\n\t"
        "v_bitop3_b32 v6, v47, s34, v6 bitop3:0xbe\n\t"
        "v_cmp_ne_u32 vcc, 0, v6\n\t"
        "s_nop 0\n\t"
        "s_cbranch_vccz .Lg2ok13\n\t"
        "s_mov_b32 s31, 65536\n\t"
        ".Lg2sp14:\n\t"
        "s_add_u32 s33, s6, 0\n\t"
        "buffer_load_dwordx4 v[16:19], v2, s[56:59], s33 offen offset:0 sc1\n\t"
        "buffer_load_dwordx4 v[20:23], v2, s[56:59], s33 offen offset:1024 sc1\n\t"
        "s_add_u32 s33, s6, 12288\n\t"
        "buffer_load_dwordx4 v[24:27], v2, s[56:59], s33 offen offset:0 sc1\n\t"
        "buffer_load_dwordx4 v[28:31], v2, s[56:59], s33 offen offset:1024 sc1\n\t"
        "s_add_u32 s33, s6, 24576\n\t"
        "buffer_load_dwordx4 v[32:35], v2, s[56:59], s33 offen offset:0 sc1\n\t"
        "buffer_load_dwordx4 v[36:39], v2, s[56:59], s33 offen offset:1024 sc1\n\t"
        "s_add_u32 s33, s6, 36864\n\t"
        "buffer_load_dwordx4 v[40:43], v2, s[56:59], s33 offen offset:0 sc1\n\t"
        "buffer_load_dwordx4 v[44:47], v2, s[56:59], s33 offen offset:1024 sc1\n\t"
        "s_waitcnt vmcnt(0)\n\t"
        "v_xor_b32 v6, s34, v17\n\t"
        "v_bitop3_b32 v6, v19, s34, v6 bitop3:0xbe\n\t"
        "v_bitop3_b32 v6, v21, s34, v6 bitop3:0xbe\n\t"
        "v_bitop3_b32 v6, v23, s34, v6 bitop3:0xbe\n\t"
        "v_bitop3_b32 v6, v25, s34, v6 bitop3:0xbe\n\t"
        "v_bitop3_b32 v6, v27, s34, v6 bitop3:0xbe\n\t"
        "v_bitop3_b32 v6, v29, s34, v6 bitop3:0xbe\n\t"
        "v_bitop3_b32 v6, v31, s34, v6 bitop3:0xbe\n\t"
        "v_bitop3_b32 v6, v33, s34, v6 bitop3:0xbe\n\t"
        "v_bitop3_b32 v6, v35, s34, v6 bitop3:0xbe\n\t"
        "v_bitop3_b32 v6, v37, s34, v6 bitop3:0xbe\n\t"
        "v_bitop3_b32 v6, v39, s34, v6 bitop3:0xbe\n\t"
        "v_bitop3_b32 v6, v41, s34, v6 bitop3:0xbe\n\t"
        "v_bitop3_b32 v6, v43, s34, v6 bitop3:0xbe\n\t"
        "v_bitop3_b32 v6, v45, s34, v6 bitop3:0xbe\n\t"
        "v_bitop3_b32 v6, v47, s34, v6 bitop3:0xbe\n\t"
        "v_cmp_ne_u32 vcc, 0, v6\n\t"
        "s_nop 0\n\t"
        "s_cbranch_vccz .Lg2ok13\n\t"
        "s_sub_u32 s31, s31, 1\n\t"
        "s_cmp_lg_u32 s31, 0\n\t"
        "s_cbranch_scc1 .Lg2sp14\n\t"
        ".Lg2ok13:\n\t"
        "v_lshlrev_b32 v80, 16, v16\n\t"
        "v_and_b32 v81, s42, v16\n\t"
        "v_lshlrev_b32 v82, 16, v18\n\t"
        "v_and_b32 v83, s42, v18\n\t"
        "v_lshlrev_b32 v6, 16, v24\n\t"
        "v_and_b32 v7, s42, v24\n\t"
        "v_pk_add_f32 v[80:81], v[80:81], v[6:7]\n\t"
        "v_lshlrev_b32 v6, 16, v26\n\t"
        "v_and_b32 v7, s42, v26\n\t"
        "v_pk_add_f32 v[82:83], v[82:83], v[6:7]\n\t"
        "v_lshlrev_b32 v6, 16, v32\n\t"
        "v_and_b32 v7, s42, v32\n\t"
        "v_pk_add_f32 v[80:81], v[80:81], v[6:7]\n\t"
        "v_lshlrev_b32 v6, 16, v34\n\t"
        "v_and_b32 v7, s42, v34\n\t"
        "v_pk_add_f32 v[82:83], v[82:83], v[6:7]\n\t"
        "v_lshlrev_b32 v6, 16, v40\n\t"
        "v_and_b32 v7, s42, v40\n\t"
        "v_pk_add_f32 v[80:81], v[80:81], v[6:7]\n\t"
        "v_lshlrev_b32 v6, 16, v42\n\t"
        "v_and_b32 v7, s42, v42\n\t"
        "v_pk_add_f32 v[82:83], v[82:83], v[6:7]\n\t"
        "v_lshlrev_b32 v84, 16, v20\n\t"
        "v_and_b32 v85, s42, v20\n\t"
        "v_lshlrev_b32 v86, 16, v22\n\t"
        "v_and_b32 v87, s42, v22\n\t"
        "v_lshlrev_b32 v6, 16, v28\n\t"
        "v_and_b32 v7, s42, v28\n\t"
        "v_pk_add_f32 v[84:85], v[84:85], v[6:7]\n\t"
        "v_lshlrev_b32 v6, 16, v30\n\t"
        "v_and_b32 v7, s42, v30\n\t"
        "v_pk_add_f32 v[86:87], v[86:87], v[6:7]\n\t"
        "v_lshlrev_b32 v6, 16, v36\n\t"
        "v_and_b32 v7, s42, v36\n\t"
        "v_pk_add_f32 v[84:85], v[84:85], v[6:7]\n\t"
        "v_lshlrev_b32 v6, 16, v38\n\t"
        "v_and_b32 v7, s42, v38\n\t"
        "v_pk_add_f32 v[86:87], v[86:87], v[6:7]\n\t"
        "v_lshlrev_b32 v6, 16, v44\n\t"
        "v_and_b32 v7, s42, v44\n\t"
        "v_pk_add_f32 v[84:85], v[84:85], v[6:7]\n\t"
        "v_lshlrev_b32 v6, 16, v46\n\t"
        "v_and_b32 v7, s42, v46\n\t"
        "v_pk_add_f32 v[86:87], v[86:87], v[6:7]\n\t"
        "v_cvt_pk_bf16_f32 v16, v80, v81\n\t"
        "v_cvt_pk_bf16_f32 v17, v82, v83\n\t"
        "v_cvt_pk_bf16_f32 v18, v84, v85\n\t"
        "v_cvt_pk_bf16_f32 v19, v86, v87\n\t"
        "s_cmp_lg_u32 s24, 2\n\t"
        "s_cbranch_scc1 .Lg2cb15\n\t"
        "v_lshlrev_b32 v6, 16, v16\n\t"
        "v_and_b32 v7, s42, v16\n\t"
        "v_add_f32 v6, 1.0, v6\n\t"
        "v_add_f32 v7, 1.0, v7\n\t"
        "v_cvt_pk_bf16_f32 v16, v6, v7\n\t"
        "v_lshlrev_b32 v6, 16, v17\n\t"
        "v_and_b32 v7, s42, v17\n\t"
        "v_add_f32 v6, 1.0, v6\n\t"
        "v_add_f32 v7, 1.0, v7\n\t"
        "v_cvt_pk_bf16_f32 v17, v6, v7\n\t"
        "v_lshlrev_b32 v6, 16, v18\n\t"
        "v_and_b32 v7, s42, v18\n\t"
        "v_add_f32 v6, 1.0, v6\n\t"
        "v_add_f32 v7, 1.0, v7\n\t"
        "v_cvt_pk_bf16_f32 v18, v6, v7\n\t"
        "v_lshlrev_b32 v6, 16, v19\n\t"
        "v_and_b32 v7, s42, v19\n\t"
        "v_add_f32 v6, 1.0, v6\n\t"
        "v_add_f32 v7, 1.0, v7\n\t"
        "v_cvt_pk_bf16_f32 v19, v6, v7\n\t"
        ".Lg2cb15:\n\t"
        "s_cmp_eq_u32 s24, 0\n\t"
        "s_cbranch_scc1 .Lg2t16\n\t"
        "s_cmp_eq_u32 s24, 3\n\t"
        "s_cbranch_scc1 .Lg2t16\n\t"
        "s_cmp_eq_u32 s24, 2\n\t"
        "s_mov_b32 s31, 152576\n\t"
        "s_cselect_b32 s31, 151552, s31\n\t"
        "v_lshrrev_b32 v6, 1, v2\n\t"
        "v_add_u32 v6, s31, v6\n\t"
        "ds_write_b64 v6, v[16:17]\n\t"
        "ds_write_b64 v6, v[18:19] offset:512\n\t"
        ".Lg2t16:\n\t"
        "s_cmp_eq_u32 s24, 1\n\t"
        "s_cbranch_scc1 .Lg2o17\n\t"
        "s_cmp_ge_u32 s27, 4\n\t"
        "s_cbranch_scc0 .Lg2l18\n\t"
        "v_mov_b32 v16, v18\n\t"
        "v_mov_b32 v17, v19\n\t"
        ".Lg2l18:\n\t"
        "v_lshlrev_b32 v20, 16, v16\n\t"
        "v_and_b32 v21, s42, v16\n\t"
        "v_lshlrev_b32 v22, 16, v17\n\t"
        "v_and_b32 v23, s42, v17\n\t"
        "s_cmp_eq_u32 s24, 0\n\t"
        "s_mov_b32 s31, 155136\n\t"
        "s_cselect_b32 s31, 154624, s31\n\t"
        "s_cmp_eq_u32 s24, 2\n\t"
        "s_cselect_b32 s31, 154880, s31\n\t"
        "v_lshl_add_u32 v6, v4, 4, s31\n\t"
        "s_and_b32 s33, s27, 3\n\t"
        "v_cmp_eq_u32 vcc, s33, v3\n\t"
        "s_nop 0\n\t"
        "s_mov_b64 exec, vcc\n\t"
        "ds_write_b128 v6, v[20:23]\n\t"
        "s_mov_b64 exec, -1\n\t"
        ".Lg2o17:\n\t"
        "s_waitcnt vmcnt(15)\n\t"
        "s_nop 0\n\t"
        "v_mfma_f32_16x16x32_bf16 v[48:51], a[192:195], a[96:99], 0\n\t"
        "v_mfma_f32_16x16x32_bf16 v[52:55], a[196:199], a[96:99], 0\n\t"
        "v_mfma_f32_16x16x32_bf16 v[56:59], a[200:203], a[96:99], 0\n\t"
        "v_mfma_f32_16x16x32_bf16 v[60:63], a[204:207], a[96:99], 0\n\t"
        "s_waitcnt vmcnt(14)\n\t"
        "v_mfma_f32_16x16x32_bf16 v[48:51], a[208:211], a[100:103], v[48:51]\n\t"
        "v_mfma_f32_16x16x32_bf16 v[52:55], a[212:215], a[100:103], v[52:55]\n\t"
        "v_mfma_f32_16x16x32_bf16 v[56:59], a[216:219], a[100:103], v[56:59]\n\t"
        "v_mfma_f32_16x16x32_bf16 v[60:63], a[220:223], a[100:103], v[60:63]\n\t"
        "s_waitcnt vmcnt(13)\n\t"
        "v_mfma_f32_16x16x32_bf16 v[48:51], a[224:227], a[104:107], v[48:51]\n\t"
        "v_mfma_f32_16x16x32_bf16 v[52:55], a[228:231], a[104:107], v[52:55]\n\t"
        "v_mfma_f32_16x16x32_bf16 v[56:59], a[232:235], a[104:107], v[56:59]\n\t"
        "v_mfma_f32_16x16x32_bf16 v[60:63], a[236:239], a[104:107], v[60:63]\n\t"
        "s_waitcnt vmcnt(12)\n\t"
        "v_mfma_f32_16x16x32_bf16 v[48:51], a[240:243], a[108:111], v[48:51]\n\t"
        "v_mfma_f32_16x16x32_bf16 v[52:55], a[244:247], a[108:111], v[52:55]\n\t"
        "v_mfma_f32_16x16x32_bf16 v[56:59], a[248:251], a[108:111], v[56:59]\n\t"
        "v_mfma_f32_16x16x32_bf16 v[60:63], a[252:255], a[108:111], v[60:63]\n\t"
        "s_waitcnt vmcnt(11)\n\t"
        "v_mfma_f32_16x16x32_bf16 v[64:67], a[192:195], a[112:115], 0\n\t"
        "v_mfma_f32_16x16x32_bf16 v[68:71], a[196:199], a[112:115], 0\n\t"
        "v_mfma_f32_16x16x32_bf16 v[72:75], a[200:203], a[112:115], 0\n\t"
        "v_mfma_f32_16x16x32_bf16 v[76:79], a[204:207], a[112:115], 0\n\t"
        "s_waitcnt vmcnt(10)\n\t"
        "v_mfma_f32_16x16x32_bf16 v[64:67], a[208:211], a[116:119], v[64:67]\n\t"
        "v_mfma_f32_16x16x32_bf16 v[68:71], a[212:215], a[116:119], v[68:71]\n\t"
        "v_mfma_f32_16x16x32_bf16 v[72:75], a[216:219], a[116:119], v[72:75]\n\t"
        "v_mfma_f32_16x16x32_bf16 v[76:79], a[220:223], a[116:119], v[76:79]\n\t"
        "s_waitcnt vmcnt(9)\n\t"
        "v_mfma_f32_16x16x32_bf16 v[64:67], a[224:227], a[120:123], v[64:67]\n\t"
        "ds_write_b128 v10, v[48:51] offset:0\n\t"
        "v_mfma_f32_16x16x32_bf16 v[68:71], a[228:231], a[120:123], v[68:71]\n\t"
        "ds_write_b128 v10, v[52:55] offset:1024\n\t"
        "v_mfma_f32_16x16x32_bf16 v[72:75], a[232:235], a[120:123], v[72:75]\n\t"
        "ds_write_b128 v10, v[56:59] offset:2048\n\t"
        "v_mfma_f32_16x16x32_bf16 v[76:79], a[236:239], a[120:123], v[76:79]\n\t"
        "ds_write_b128 v10, v[60:63] offset:3072\n\t"
        "s_waitcnt vmcnt(8)\n\t"
        "v_mfma_f32_16x16x32_bf16 v[64:67], a[240:243], a[124:127], v[64:67]\n\t"
        "v_mfma_f32_16x16x32_bf16 v[68:71], a[244:247], a[124:127], v[68:71]\n\t"
        "v_mfma_f32_16x16x32_bf16 v[72:75], a[248:251], a[124:127], v[72:75]\n\t"
        "v_mfma_f32_16x16x32_bf16 v[76:79], a[252:255], a[124:127], v[76:79]\n\t"
        "s_waitcnt vmcnt(7)\n\t"
        "v_mfma_f32_16x16x32_bf16 v[48:51], a[192:195], a[128:131], 0\n\t"
        "v_mfma_f32_16x16x32_bf16 v[52:55], a[196:199], a[128:131], 0\n\t"
        "v_mfma_f32_16x16x32_bf16 v[56:59], a[200:203], a[128:131], 0\n\t"
        "v_mfma_f32_16x16x32_bf16 v[60:63], a[204:207], a[128:131], 0\n\t"
        "s_waitcnt vmcnt(6)\n\t"
        "v_mfma_f32_16x16x32_bf16 v[48:51], a[208:211], a[132:135], v[48:51]\n\t"
        "v_mfma_f32_16x16x32_bf16 v[52:55], a[212:215], a[132:135], v[52:55]\n\t"
        "v_mfma_f32_16x16x32_bf16 v[56:59], a[216:219], a[132:135], v[56:59]\n\t"
        "v_mfma_f32_16x16x32_bf16 v[60:63], a[220:223], a[132:135], v[60:63]\n\t"
        "s_waitcnt vmcnt(5)\n\t"
        "v_mfma_f32_16x16x32_bf16 v[48:51], a[224:227], a[136:139], v[48:51]\n\t"
        "ds_write_b128 v11, v[64:67] offset:0\n\t"
        "v_mfma_f32_16x16x32_bf16 v[52:55], a[228:231], a[136:139], v[52:55]\n\t"
        "ds_write_b128 v11, v[68:71] offset:1024\n\t"
        "v_mfma_f32_16x16x32_bf16 v[56:59], a[232:235], a[136:139], v[56:59]\n\t"
        "ds_write_b128 v11, v[72:75] offset:2048\n\t"
        "v_mfma_f32_16x16x32_bf16 v[60:63], a[236:239], a[136:139], v[60:63]\n\t"
        "ds_write_b128 v11, v[76:79] offset:3072\n\t"
        "s_waitcnt vmcnt(4)\n\t"
        "v_mfma_f32_16x16x32_bf16 v[48:51], a[240:243], a[140:143], v[48:51]\n\t"
        "v_mfma_f32_16x16x32_bf16 v[52:55], a[244:247], a[140:143], v[52:55]\n\t"
        "v_mfma_f32_16x16x32_bf16 v[56:59], a[248:251], a[140:143], v[56:59]\n\t"
        "v_mfma_f32_16x16x32_bf16 v[60:63], a[252:255], a[140:143], v[60:63]\n\t"
        "s_waitcnt vmcnt(3)\n\t"
        "v_mfma_f32_16x16x32_bf16 v[64:67], a[192:195], a[144:147], 0\n\t"
        "v_mfma_f32_16x16x32_bf16 v[68:71], a[196:199], a[144:147], 0\n\t"
        "v_mfma_f32_16x16x32_bf16 v[72:75], a[200:203], a[144:147], 0\n\t"
        "v_mfma_f32_16x16x32_bf16 v[76:79], a[204:207], a[144:147], 0\n\t"
        "s_waitcnt vmcnt(2)\n\t"
        "v_mfma_f32_16x16x32_bf16 v[64:67], a[208:211], a[148:151], v[64:67]\n\t"
        "v_mfma_f32_16x16x32_bf16 v[68:71], a[212:215], a[148:151], v[68:71]\n\t"
        "v_mfma_f32_16x16x32_bf16 v[72:75], a[216:219], a[148:151], v[72:75]\n\t"
        "v_mfma_f32_16x16x32_bf16 v[76:79], a[220:223], a[148:151], v[76:79]\n\t"
        "s_waitcnt vmcnt(1)\n\t"
        "v_mfma_f32_16x16x32_bf16 v[64:67], a[224:227], a[152:155], v[64:67]\n\t"
        "ds_write_b128 v12, v[48:51] offset:0\n\t"
        "v_mfma_f32_16x16x32_bf16 v[68:71], a[228:231], a[152:155], v[68:71]\n\t"
        "ds_write_b128 v12, v[52:55] offset:1024\n\t"
        "v_mfma_f32_16x16x32_bf16 v[72:75], a[232:235], a[152:155], v[72:75]\n\t"
        "ds_write_b128 v12, v[56:59] offset:2048\n\t"
        "v_mfma_f32_16x16x32_bf16 v[76:79], a[236:239], a[152:155], v[76:79]\n\t"
        "ds_write_b128 v12, v[60:63] offset:3072\n\t"
        "s_waitcnt vmcnt(0)\n\t"
        "v_mfma_f32_16x16x32_bf16 v[64:67], a[240:243], a[156:159], v[64:67]\n\t"
        "v_mfma_f32_16x16x32_bf16 v[68:71], a[244:247], a[156:159], v[68:71]\n\t"
        "v_mfma_f32_16x16x32_bf16 v[72:75], a[248:251], a[156:159], v[72:75]\n\t"
        "v_mfma_f32_16x16x32_bf16 v[76:79], a[252:255], a[156:159], v[76:79]\n\t"
        "s_waitcnt lgkmcnt(0)\n\t"
        "s_barrier\n\t"
        "ds_read_b128 v[48:51], v13 offset:0\n\t"
        "ds_read_b128 v[52:55], v13 offset:1024\n\t"
        "ds_read_b128 v[56:59], v13 offset:2048\n\t"
        "ds_read_b128 v[60:63], v13 offset:3072\n\t"
        "ds_read_b128 v[16:19], v13 offset:4096\n\t"
        "ds_read_b128 v[20:23], v13 offset:5120\n\t"
        "ds_read_b128 v[24:27], v13 offset:6144\n\t"
        "ds_read_b128 v[28:31], v13 offset:7168\n\t"
        "s_waitcnt lgkmcnt(7)\n\t"
        "v_pk_add_f32 v[64:65], v[64:65], v[48:49]\n\t"
        "v_pk_add_f32 v[66:67], v[66:67], v[50:51]\n\t"
        "s_waitcnt lgkmcnt(6)\n\t"
        "v_pk_add_f32 v[68:69], v[68:69], v[52:53]\n\t"
        "v_pk_add_f32 v[70:71], v[70:71], v[54:55]\n\t"
        "s_waitcnt lgkmcnt(5)\n\t"
        "v_pk_add_f32 v[72:73], v[72:73], v[56:57]\n\t"
        "v_pk_add_f32 v[74:75], v[74:75], v[58:59]\n\t"
        "s_waitcnt lgkmcnt(4)\n\t"
        "v_pk_add_f32 v[76:77], v[76:77], v[60:61]\n\t"
        "v_pk_add_f32 v[78:79], v[78:79], v[62:63]\n\t"
        "ds_read_b128 v[48:51], v13 offset:8192\n\t"
        "ds_read_b128 v[52:55], v13 offset:9216\n\t"
        "ds_read_b128 v[56:59], v13 offset:10240\n\t"
        "ds_read_b128 v[60:63], v13 offset:11264\n\t"
        "s_waitcnt lgkmcnt(7)\n\t"
        "v_pk_add_f32 v[64:65], v[64:65], v[16:17]\n\t"
        "v_pk_add_f32 v[66:67], v[66:67], v[18:19]\n\t"
        "s_waitcnt lgkmcnt(6)\n\t"
        "v_pk_add_f32 v[68:69], v[68:69], v[20:21]\n\t"
        "v_pk_add_f32 v[70:71], v[70:71], v[22:23]\n\t"
        "s_waitcnt lgkmcnt(5)\n\t"
        "v_pk_add_f32 v[72:73], v[72:73], v[24:25]\n\t"
        "v_pk_add_f32 v[74:75], v[74:75], v[26:27]\n\t"
        "s_waitcnt lgkmcnt(4)\n\t"
        "v_pk_add_f32 v[76:77], v[76:77], v[28:29]\n\t"
        "v_pk_add_f32 v[78:79], v[78:79], v[30:31]\n\t"
        "s_waitcnt lgkmcnt(3)\n\t"
        "v_pk_add_f32 v[64:65], v[64:65], v[48:49]\n\t"
        "v_pk_add_f32 v[66:67], v[66:67], v[50:51]\n\t"
        "s_waitcnt lgkmcnt(2)\n\t"
        "v_pk_add_f32 v[68:69], v[68:69], v[52:53]\n\t"
        "v_pk_add_f32 v[70:71], v[70:71], v[54:55]\n\t"
        "s_waitcnt lgkmcnt(1)\n\t"
        "v_pk_add_f32 v[72:73], v[72:73], v[56:57]\n\t"
        "v_pk_add_f32 v[74:75], v[74:75], v[58:59]\n\t"
        "s_waitcnt lgkmcnt(0)\n\t"
        "v_pk_add_f32 v[76:77], v[76:77], v[60:61]\n\t"
        "v_pk_add_f32 v[78:79], v[78:79], v[62:63]\n\t"
        "v_permlane16_swap_b32 v64, v68\n\t"
        "v_permlane16_swap_b32 v65, v69\n\t"
        "v_permlane16_swap_b32 v66, v70\n\t"
        "v_permlane16_swap_b32 v67, v71\n\t"
        "v_permlane16_swap_b32 v72, v76\n\t"
        "v_permlane16_swap_b32 v73, v77\n\t"
        "v_permlane16_swap_b32 v74, v78\n\t"
        "v_permlane16_swap_b32 v75, v79\n\t"
        "v_add_u32 v6, 154624, v140\n\t"
        "ds_read_b128 v[80:83], v6\n\t"
        "ds_read_b128 v[84:87], v6 offset:16\n\t"
        "ds_read_b128 v[88:91], v6 offset:256\n\t"
        "ds_read_b128 v[92:95], v6 offset:272\n\t"
        "v_add_u32 v7, 154752, v140\n\t"
        "ds_read_b128 v[96:99], v7\n\t"
        "ds_read_b128 v[100:103], v7 offset:16\n\t"
        "ds_read_b128 v[104:107], v7 offset:256\n\t"
        "ds_read_b128 v[108:111], v7 offset:272\n\t"
        "s_lshl_b32 s31, s29, 2\n\t"
        "s_add_u32 s31, s31, s24\n\t"
        "s_lshl_b32 s6, s31, 14\n\t"
        "s_lshl_b32 s36, s27, 11\n\t"
        "s_add_u32 s6, s6, s36\n\t"
        "s_add_u32 s6, s6, 7340032\n\t"
        "s_lshl_b32 s7, s31, 10\n\t"
        "s_lshl_b32 s36, s27, 7\n\t"
        "s_add_u32 s7, s7, s36\n\t"
        "s_add_u32 s7, s7, 131072\n\t"
        "v_lshlrev_b32 v36, 16, v144\n\t"
        "v_and_b32 v37, s42, v144\n\t"
        "s_waitcnt lgkmcnt(7)\n\t"
        "v_pk_fma_f32 v[36:37], v[80:81], v[64:65], v[36:37]\n\t"
        "v_cvt_pk_bf16_f32 v16, v36, v37\n\t"
        "v_lshlrev_b32 v38, 16, v16\n\t"
        "v_and_b32 v39, s42, v16\n\t"
        "v_pk_mul_f32 v[34:35], v[38:39], v[38:39]\n\t"
        "v_mov_b32 v32, v38\n\t"
        "v_mov_b32 v33, v39\n\t"
        "s_waitcnt lgkmcnt(5)\n\t"
        "v_pk_mul_f32 v[38:39], v[38:39], v[88:89]\n\t"
        "v_cvt_pk_bf16_f32 v24, v38, v39\n\t"
        "v_lshlrev_b32 v36, 16, v145\n\t"
        "v_and_b32 v37, s42, v145\n\t"
        "v_pk_fma_f32 v[36:37], v[82:83], v[66:67], v[36:37]\n\t"
        "v_cvt_pk_bf16_f32 v17, v36, v37\n\t"
        "v_lshlrev_b32 v38, 16, v17\n\t"
        "v_and_b32 v39, s42, v17\n\t"
        "v_pk_fma_f32 v[34:35], v[38:39], v[38:39], v[34:35]\n\t"
        "v_pk_add_f32 v[32:33], v[32:33], v[38:39]\n\t"
        "v_pk_mul_f32 v[38:39], v[38:39], v[90:91]\n\t"
        "v_cvt_pk_bf16_f32 v25, v38, v39\n\t"
        "v_lshlrev_b32 v36, 16, v146\n\t"
        "v_and_b32 v37, s42, v146\n\t"
        "v_pk_fma_f32 v[36:37], v[84:85], v[68:69], v[36:37]\n\t"
        "v_cvt_pk_bf16_f32 v18, v36, v37\n\t"
        "v_lshlrev_b32 v38, 16, v18\n\t"
        "v_and_b32 v39, s42, v18\n\t"
        "v_pk_fma_f32 v[34:35], v[38:39], v[38:39], v[34:35]\n\t"
        "v_pk_add_f32 v[32:33], v[32:33], v[38:39]\n\t"
        "s_waitcnt lgkmcnt(4)\n\t"
        "v_pk_mul_f32 v[38:39], v[38:39], v[92:93]\n\t"
        "v_cvt_pk_bf16_f32 v26, v38, v39\n\t"
        "v_lshlrev_b32 v36, 16, v147\n\t"
        "v_and_b32 v37, s42, v147\n\t"
        "v_pk_fma_f32 v[36:37], v[86:87], v[70:71], v[36:37]\n\t"
        "v_cvt_pk_bf16_f32 v19, v36, v37\n\t"
        "v_lshlrev_b32 v38, 16, v19\n\t"
        "v_and_b32 v39, s42, v19\n\t"
        "v_pk_fma_f32 v[34:35], v[38:39], v[38:39], v[34:35]\n\t"
        "v_pk_add_f32 v[32:33], v[32:33], v[38:39]\n\t"
        "v_pk_mul_f32 v[38:39], v[38:39], v[94:95]\n\t"
        "v_cvt_pk_bf16_f32 v27, v38, v39\n\t"
        "buffer_store_dwordx4 v[24:27], v141, s[56:59], s6 offen offset:0 sc1\n\t"
        "v_lshlrev_b32 v36, 16, v148\n\t"
        "v_and_b32 v37, s42, v148\n\t"
        "s_waitcnt lgkmcnt(3)\n\t"
        "v_pk_fma_f32 v[36:37], v[96:97], v[72:73], v[36:37]\n\t"
        "v_cvt_pk_bf16_f32 v20, v36, v37\n\t"
        "v_lshlrev_b32 v38, 16, v20\n\t"
        "v_and_b32 v39, s42, v20\n\t"
        "v_pk_fma_f32 v[34:35], v[38:39], v[38:39], v[34:35]\n\t"
        "v_pk_add_f32 v[32:33], v[32:33], v[38:39]\n\t"
        "s_waitcnt lgkmcnt(1)\n\t"
        "v_pk_mul_f32 v[38:39], v[38:39], v[104:105]\n\t"
        "v_cvt_pk_bf16_f32 v28, v38, v39\n\t"
        "v_lshlrev_b32 v36, 16, v149\n\t"
        "v_and_b32 v37, s42, v149\n\t"
        "v_pk_fma_f32 v[36:37], v[98:99], v[74:75], v[36:37]\n\t"
        "v_cvt_pk_bf16_f32 v21, v36, v37\n\t"
        "v_lshlrev_b32 v38, 16, v21\n\t"
        "v_and_b32 v39, s42, v21\n\t"
        "v_pk_fma_f32 v[34:35], v[38:39], v[38:39], v[34:35]\n\t"
        "v_pk_add_f32 v[32:33], v[32:33], v[38:39]\n\t"
        "v_pk_mul_f32 v[38:39], v[38:39], v[106:107]\n\t"
        "v_cvt_pk_bf16_f32 v29, v38, v39\n\t"
        "v_lshlrev_b32 v36, 16, v150\n\t"
        "v_and_b32 v37, s42, v150\n\t"
        "v_pk_fma_f32 v[36:37], v[100:101], v[76:77], v[36:37]\n\t"
        "v_cvt_pk_bf16_f32 v22, v36, v37\n\t"
        "v_lshlrev_b32 v38, 16, v22\n\t"
        "v_and_b32 v39, s42, v22\n\t"
        "v_pk_fma_f32 v[34:35], v[38:39], v[38:39], v[34:35]\n\t"
        "v_pk_add_f32 v[32:33], v[32:33], v[38:39]\n\t"
        "s_waitcnt lgkmcnt(0)\n\t"
        "v_pk_mul_f32 v[38:39], v[38:39], v[108:109]\n\t"
        "v_cvt_pk_bf16_f32 v30, v38, v39\n\t"
        "v_lshlrev_b32 v36, 16, v151\n\t"
        "v_and_b32 v37, s42, v151\n\t"
        "v_pk_fma_f32 v[36:37], v[102:103], v[78:79], v[36:37]\n\t"
        "v_cvt_pk_bf16_f32 v23, v36, v37\n\t"
        "v_lshlrev_b32 v38, 16, v23\n\t"
        "v_and_b32 v39, s42, v23\n\t"
        "v_pk_fma_f32 v[34:35], v[38:39], v[38:39], v[34:35]\n\t"
        "v_pk_add_f32 v[32:33], v[32:33], v[38:39]\n\t"
        "v_pk_mul_f32 v[38:39], v[38:39], v[110:111]\n\t"
        "v_cvt_pk_bf16_f32 v31, v38, v39\n\t"
        "buffer_store_dwordx4 v[28:31], v141, s[56:59], s6 offen offset:1024 sc1\n\t"
        "v_add_f32 v32, v32, v33\n\t"
        "v_add_f32 v34, v34, v35\n\t"
        "v_lshlrev_b32 v6, 2, v1\n\t"
        "s_nop 0\n\t"
        "v_permlane16_swap_b32 v32, v34\n\t"
        "v_add_f32 v32, v32, v34\n\t"
        "v_mov_b32 v34, v32\n\t"
        "s_nop 1\n\t"
        "v_permlane32_swap_b32 v32, v34\n\t"
        "v_add_f32 v32, v32, v34\n\t"
        "s_mov_b32 exec_hi, 0\n\t"
        "buffer_store_dword v32, v6, s[56:59], s7 offen sc1\n\t"
        "s_mov_b64 exec, -1\n\t"
        "s_mul_i32 s6, s27, 43\n\t"
        "s_mul_i32 s33, s24, 11\n\t"
        "s_add_u32 s6, s6, s33\n\t"
        "s_lshl_b32 s6, s6, 12\n\t"
        "s_add_u32 s6, s6, 2981888\n\t"
        "s_cmp_eq_u32 s24, 3\n\t"
        "s_mov_b32 s31, 0\n\t"
        "s_cselect_b32 s31, 0x7f000000, s31\n\t"
        "v_add_u32 v40, s31, v2\n\t"
        "s_add_u32 s13, s13, 4096\n\t"
        "buffer_load_dwordx4 a[64:67], v2, s[56:59], s13 offen offset:0 sc1\n\t"
        "buffer_load_dwordx4 a[68:71], v2, s[56:59], s13 offen offset:1024 sc1\n\t"
        "buffer_load_dwordx4 a[72:75], v2, s[56:59], s13 offen offset:2048 sc1\n\t"
        "buffer_load_dwordx4 a[76:79], v2, s[56:59], s13 offen offset:3072 sc1\n\t"
        "s_add_u32 s13, s13, 4096\n\t"
        "buffer_load_dwordx4 a[80:83], v2, s[56:59], s13 offen offset:0 sc1\n\t"
        "buffer_load_dwordx4 a[84:87], v2, s[56:59], s13 offen offset:1024 sc1\n\t"
        "buffer_load_dwordx4 a[88:91], v2, s[56:59], s13 offen offset:2048 sc1\n\t"
        "buffer_load_dwordx4 a[92:95], v2, s[56:59], s13 offen offset:3072 sc1\n\t"
        "buffer_load_dwordx4 a[96:99], v2, s[56:59], s6 offen offset:0 sc1\n\t"
        "buffer_load_dwordx4 a[100:103], v2, s[56:59], s6 offen offset:1024 sc1\n\t"
        "buffer_load_dwordx4 a[104:107], v2, s[56:59], s6 offen offset:2048 sc1\n\t"
        "buffer_load_dwordx4 a[108:111], v2, s[56:59], s6 offen offset:3072 sc1\n\t"
        "s_add_u32 s6, s6, 4096\n\t"
        "buffer_load_dwordx4 a[112:115], v2, s[56:59], s6 offen offset:0 sc1\n\t"
        "buffer_load_dwordx4 a[116:119], v2, s[56:59], s6 offen offset:1024 sc1\n\t"
        "buffer_load_dwordx4 a[120:123], v2, s[56:59], s6 offen offset:2048 sc1\n\t"
        "buffer_load_dwordx4 a[124:127], v2, s[56:59], s6 offen offset:3072 sc1\n\t"
        "s_add_u32 s6, s6, 4096\n\t"
        "buffer_load_dwordx4 a[128:131], v2, s[56:59], s6 offen offset:0 sc1\n\t"
        "buffer_load_dwordx4 a[132:135], v2, s[56:59], s6 offen offset:1024 sc1\n\t"
        "buffer_load_dwordx4 a[136:139], v2, s[56:59], s6 offen offset:2048 sc1\n\t"
        "buffer_load_dwordx4 a[140:143], v2, s[56:59], s6 offen offset:3072 sc1\n\t"
        "s_waitcnt vmcnt(20)\n\t"
        "s_barrier\n\t"
        "s_cmp_lg_u32 s24, 0\n\t"
        "s_cbranch_scc1 .Lpub19\n\t"
        "s_add_u32 s31, s5, 4096\n\t"
        "v_mov_b32 v6, 0\n\t"
        "s_mov_b64 exec, 1\n\t"
        "buffer_store_dwordx2 v[14:15], v6, s[56:59], s31 offen sc1\n\t"
        "s_mov_b64 exec, -1\n\t"
        ".Lpub19:\n\t"
        "v_min_u32 v6, 2, v4\n\t"
        "v_lshlrev_b32 v6, 10, v6\n\t"
        "v_lshl_add_u32 v6, v3, 4, v6\n\t"
        "v_add_u32 v6, 151552, v6\n\t"
        "ds_read_b128 v[68:71], v6\n\t"
        "s_waitcnt lgkmcnt(0)\n\t"
        "v_mfma_f32_16x16x32_bf16 v[56:59], v[68:71], v[160:163], 0\n\t"
        "ds_read_b128 v[72:75], v6 offset:64\n\t"
        "v_mfma_f32_16x16x32_bf16 v[60:63], v[68:71], v[224:227], 0\n\t"
        "v_mfma_f32_16x16x32_bf16 v[64:67], v[68:71], a[32:35], 0\n\t"
        "s_waitcnt lgkmcnt(0)\n\t"
        "v_mfma_f32_16x16x32_bf16 v[56:59], v[72:75], v[164:167], v[56:59]\n\t"
        "ds_read_b128 v[68:71], v6 offset:128\n\t"
        "v_mfma_f32_16x16x32_bf16 v[60:63], v[72:75], v[228:231], v[60:63]\n\t"
        "v_mfma_f32_16x16x32_bf16 v[64:67], v[72:75], a[36:39], v[64:67]\n\t"
        "s_waitcnt lgkmcnt(0)\n\t"
        "v_mfma_f32_16x16x32_bf16 v[56:59], v[68:71], v[168:171], v[56:59]\n\t"
        "ds_read_b128 v[72:75], v6 offset:192\n\t"
        "v_mfma_f32_16x16x32_bf16 v[60:63], v[68:71], v[232:235], v[60:63]\n\t"
        "v_mfma_f32_16x16x32_bf16 v[64:67], v[68:71], a[40:43], v[64:67]\n\t"
        "s_waitcnt lgkmcnt(0)\n\t"
        "v_mfma_f32_16x16x32_bf16 v[56:59], v[72:75], v[172:175], v[56:59]\n\t"
        "ds_read_b128 v[68:71], v6 offset:256\n\t"
        "v_mfma_f32_16x16x32_bf16 v[60:63], v[72:75], v[236:239], v[60:63]\n\t"
        "v_mfma_f32_16x16x32_bf16 v[64:67], v[72:75], a[44:47], v[64:67]\n\t"
        "s_waitcnt lgkmcnt(0)\n\t"
        "v_mfma_f32_16x16x32_bf16 v[56:59], v[68:71], v[176:179], v[56:59]\n\t"
        "ds_read_b128 v[72:75], v6 offset:320\n\t"
        "v_mfma_f32_16x16x32_bf16 v[60:63], v[68:71], v[240:243], v[60:63]\n\t"
        "v_mfma_f32_16x16x32_bf16 v[64:67], v[68:71], a[48:51], v[64:67]\n\t"
        "s_waitcnt lgkmcnt(0)\n\t"
        "v_mfma_f32_16x16x32_bf16 v[56:59], v[72:75], v[180:183], v[56:59]\n\t"
        "ds_read_b128 v[68:71], v6 offset:384\n\t"
        "v_mfma_f32_16x16x32_bf16 v[60:63], v[72:75], v[244:247], v[60:63]\n\t"
        "v_mfma_f32_16x16x32_bf16 v[64:67], v[72:75], a[52:55], v[64:67]\n\t"
        "s_waitcnt lgkmcnt(0)\n\t"
        "v_mfma_f32_16x16x32_bf16 v[56:59], v[68:71], v[184:187], v[56:59]\n\t"
        "ds_read_b128 v[72:75], v6 offset:448\n\t"
        "v_mfma_f32_16x16x32_bf16 v[60:63], v[68:71], v[248:251], v[60:63]\n\t"
        "v_mfma_f32_16x16x32_bf16 v[64:67], v[68:71], a[56:59], v[64:67]\n\t"
        "s_waitcnt lgkmcnt(0)\n\t"
        "v_mfma_f32_16x16x32_bf16 v[56:59], v[72:75], v[188:191], v[56:59]\n\t"
        "ds_read_b128 v[68:71], v6 offset:512\n\t"
        "v_mfma_f32_16x16x32_bf16 v[60:63], v[72:75], v[252:255], v[60:63]\n\t"
        "v_mfma_f32_16x16x32_bf16 v[64:67], v[72:75], a[60:63], v[64:67]\n\t"
        "s_waitcnt lgkmcnt(0)\n\t"
        "v_mfma_f32_16x16x32_bf16 v[56:59], v[68:71], v[192:195], v[56:59]\n\t"
        "ds_read_b128 v[72:75], v6 offset:576\n\t"
        "v_mfma_f32_16x16x32_bf16 v[60:63], v[68:71], a[0:3], v[60:63]\n\t"
        "s_waitcnt vmcnt(19)\n\t"
        "v_mfma_f32_16x16x32_bf16 v[64:67], v[68:71], a[64:67], v[64:67]\n\t"
        "s_waitcnt lgkmcnt(0)\n\t"
        "v_mfma_f32_16x16x32_bf16 v[56:59], v[72:75], v[196:199], v[56:59]\n\t"
        "ds_read_b128 v[68:71], v6 offset:640\n\t"
        "v_mfma_f32_16x16x32_bf16 v[60:63], v[72:75], a[4:7], v[60:63]\n\t"
        "s_waitcnt vmcnt(18)\n\t"
        "v_mfma_f32_16x16x32_bf16 v[64:67], v[72:75], a[68:71], v[64:67]\n\t"
        "s_waitcnt lgkmcnt(0)\n\t"
        "v_mfma_f32_16x16x32_bf16 v[56:59], v[68:71], v[200:203], v[56:59]\n\t"
        "ds_read_b128 v[72:75], v6 offset:704\n\t"
        "v_mfma_f32_16x16x32_bf16 v[60:63], v[68:71], a[8:11], v[60:63]\n\t"
        "s_waitcnt vmcnt(17)\n\t"
        "v_mfma_f32_16x16x32_bf16 v[64:67], v[68:71], a[72:75], v[64:67]\n\t"
        "s_waitcnt lgkmcnt(0)\n\t"
        "v_mfma_f32_16x16x32_bf16 v[56:59], v[72:75], v[204:207], v[56:59]\n\t"
        "ds_read_b128 v[68:71], v6 offset:768\n\t"
        "v_mfma_f32_16x16x32_bf16 v[60:63], v[72:75], a[12:15], v[60:63]\n\t"
        "s_waitcnt vmcnt(16)\n\t"
        "v_mfma_f32_16x16x32_bf16 v[64:67], v[72:75], a[76:79], v[64:67]\n\t"
        "s_waitcnt lgkmcnt(0)\n\t"
        "v_mfma_f32_16x16x32_bf16 v[56:59], v[68:71], v[208:211], v[56:59]\n\t"
        "ds_read_b128 v[72:75], v6 offset:832\n\t"
        "v_mfma_f32_16x16x32_bf16 v[60:63], v[68:71], a[16:19], v[60:63]\n\t"
        "s_waitcnt vmcnt(15)\n\t"
        "v_mfma_f32_16x16x32_bf16 v[64:67], v[68:71], a[80:83], v[64:67]\n\t"
        "s_waitcnt lgkmcnt(0)\n\t"
        "v_mfma_f32_16x16x32_bf16 v[56:59], v[72:75], v[212:215], v[56:59]\n\t"
        "ds_read_b128 v[68:71], v6 offset:896\n\t"
        "v_mfma_f32_16x16x32_bf16 v[60:63], v[72:75], a[20:23], v[60:63]\n\t"
        "s_waitcnt vmcnt(14)\n\t"
        "v_mfma_f32_16x16x32_bf16 v[64:67], v[72:75], a[84:87], v[64:67]\n\t"
        "s_waitcnt lgkmcnt(0)\n\t"
        "v_mfma_f32_16x16x32_bf16 v[56:59], v[68:71], v[216:219], v[56:59]\n\t"
        "ds_read_b128 v[72:75], v6 offset:960\n\t"
        "v_mfma_f32_16x16x32_bf16 v[60:63], v[68:71], a[24:27], v[60:63]\n\t"
        "s_waitcnt vmcnt(13)\n\t"
        "v_mfma_f32_16x16x32_bf16 v[64:67], v[68:71], a[88:91], v[64:67]\n\t"
        "s_waitcnt lgkmcnt(0)\n\t"
        "v_mfma_f32_16x16x32_bf16 v[56:59], v[72:75], v[220:223], v[56:59]\n\t"
        "v_mfma_f32_16x16x32_bf16 v[60:63], v[72:75], a[28:31], v[60:63]\n\t"
        "s_waitcnt vmcnt(12)\n\t"
        "v_mfma_f32_16x16x32_bf16 v[64:67], v[72:75], a[92:95], v[64:67]\n\t"
        "s_nop 4\n\t"
        "v_cvt_pk_bf16_f32 v45, v56, v57\n\t"
        "v_lshlrev_b32 v6, 16, v45\n\t"
        "v_and_b32 v7, s42, v45\n\t"
        "v_sub_f32 v6, v56, v6\n\t"
        "v_sub_f32 v7, v57, v7\n\t"
        "v_cvt_pk_bf16_f32 v44, v56, v6\n\t"
        "v_cvt_pk_bf16_f32 v46, v7, v57\n\t"
        "v_mov_b32 v47, 0\n\t"
        "v_cvt_pk_bf16_f32 v49, v60, v61\n\t"
        "v_lshlrev_b32 v6, 16, v49\n\t"
        "v_and_b32 v7, s42, v49\n\t"
        "v_sub_f32 v6, v60, v6\n\t"
        "v_sub_f32 v7, v61, v7\n\t"
        "v_cvt_pk_bf16_f32 v48, v60, v6\n\t"
        "v_cvt_pk_bf16_f32 v50, v7, v61\n\t"
        "v_mov_b32 v51, 0\n\t"
        "v_cvt_pk_bf16_f32 v53, v64, v65\n\t"
        "v_lshlrev_b32 v6, 16, v53\n\t"
        "v_and_b32 v7, s42, v53\n\t"
        "v_sub_f32 v6, v64, v6\n\t"
        "v_sub_f32 v7, v65, v7\n\t"
        "v_cvt_pk_bf16_f32 v52, v64, v6\n\t"
        "v_cvt_pk_bf16_f32 v54, v7, v65\n\t"
        "v_mov_b32 v55, 0\n\t"
        "s_lshl_b32 s31, s29, 2\n\t"
        "s_add_u32 s31, s31, s24\n\t"
        "s_lshl_b32 s8, s31, 10\n\t"
        "s_add_u32 s8, s8, 131072\n\t"
        "s_lshl_b32 s7, s29, 16\n\t"
        "s_lshl_b32 s33, s24, 12\n\t"
        "s_add_u32 s7, s7, s33\n\t"
        "s_add_u32 s7, s7, 7340032\n\t"
        "s_add_u32 s33, s33, 65536\n\t"
        "v_add_u32 v41, s33, v2\n\t"
        "v_add_u32 v42, 65536, v2\n\t"
        "s_mul_i32 s19, s29, 176128\n\t"
        "s_add_u32 s19, s19, 9437184\n\t"
        "v_lshlrev_b32 v43, 2, v4\n\t"
        "v_add_u32 v43, 155648, v43\n\t"
        "v_and_b32 v6, 1, v3\n\t"
        "v_add_u32 v7, s9, v6\n\t"
        "v_lshrrev_b32 v8, 1, v7\n\t"
        "v_lshlrev_b32 v8, 10, v8\n\t"
        "v_and_b32 v7, 1, v7\n\t"
        "v_lshlrev_b32 v7, 1, v7\n\t"
        "v_lshrrev_b32 v9, 1, v3\n\t"
        "v_add_u32 v7, v7, v9\n\t"
        "v_lshl_add_u32 v7, v7, 4, v4\n\t"
        "v_lshl_add_u32 v92, v7, 4, v8\n\t"
        "s_add_u32 s31, s9, 2\n\t"
        "s_lshr_b32 s33, s31, 1\n\t"
        "s_lshl_b32 s33, s33, 10\n\t"
        "s_and_b32 s31, s31, 1\n\t"
        "s_lshl_b32 s31, s31, 9\n\t"
        "s_add_u32 s31, s31, s33\n\t"
        "v_mul_u32_u24 v6, 44032, v6\n\t"
        "v_lshl_add_u32 v9, v9, 4, v4\n\t"
        "v_lshl_add_u32 v6, v9, 4, v6\n\t"
        "v_add_u32 v93, s31, v6\n\t"
        "s_cmp_eq_u32 s10, 3\n\t"
        "s_mov_b32 s31, 0x7f000000\n\t"
        "s_cselect_b32 s31, 0, s31\n\t"
        "v_add_u32 v93, s31, v93\n\t"
        "s_cmp_lg_u32 s24, 0\n\t"
        "s_cbranch_scc1 .Lwf20_done\n\t"
        "v_and_b32 v6, 7, v1\n\t"
        "v_lshlrev_b32 v6, 3, v6\n\t"
        "s_add_u32 s33, s4, 4096\n\t"
        "s_mov_b32 s31, 65536\n\t"
        ".Lwf20_spin:\n\t"
        "buffer_load_dwordx2 v[8:9], v6, s[56:59], s33 offen sc1\n\t"
        "s_waitcnt vmcnt(0)\n\t"
        "v_cmp_ne_u64 vcc, s[34:35], v[8:9]\n\t"
        "s_nop 0\n\t"
        "s_cbranch_vccz .Lwf20_done\n\t"
        "s_sub_u32 s31, s31, 1\n\t"
        "s_cmp_lg_u32 s31, 0\n\t"
        "s_cbranch_scc1 .Lwf20_spin\n\t"
        ".Lwf20_done:\n\t"
        "s_barrier\n\t"
        "s_add_u32 s36, s7, 0\n\t"
        "buffer_load_dwordx4 v[56:59], v2, s[56:59], s36 offen offset:0 sc1\n\t"
        "buffer_load_dwordx4 v[60:63], v2, s[56:59], s36 offen offset:1024 sc1\n\t"
        "buffer_load_dwordx4 v[64:67], v2, s[56:59], s36 offen offset:2048 sc1\n\t"
        "buffer_load_dwordx4 v[68:71], v2, s[56:59], s36 offen offset:3072 sc1\n\t"
        "buffer_load_dwordx4 v[88:91], v2, s[56:59], s8 offen sc1\n\t"
        "s_add_u32 s36, s7, 16384\n\t"
        "buffer_load_dwordx4 v[72:75], v2, s[56:59], s36 offen offset:0 sc1\n\t"
        "buffer_load_dwordx4 v[76:79], v2, s[56:59], s36 offen offset:1024 sc1\n\t"
        "buffer_load_dwordx4 v[80:83], v2, s[56:59], s36 offen offset:2048 sc1\n\t"
        "buffer_load_dwordx4 v[84:87], v2, s[56:59], s36 offen offset:3072 sc1\n\t"
        "s_waitcnt vmcnt(8)\n\t"
        "ds_write_b128 v41, v[56:59] offset:0\n\t"
        "s_waitcnt vmcnt(7)\n\t"
        "ds_write_b128 v41, v[60:63] offset:1024\n\t"
        "s_waitcnt vmcnt(6)\n\t"
        "ds_write_b128 v41, v[64:67] offset:2048\n\t"
        "s_waitcnt vmcnt(5)\n\t"
        "ds_write_b128 v41, v[68:71] offset:3072\n\t"
        "s_add_u32 s36, s7, 32768\n\t"
        "buffer_load_dwordx4 v[56:59], v2, s[56:59], s36 offen offset:0 sc1\n\t"
        "buffer_load_dwordx4 v[60:63], v2, s[56:59], s36 offen offset:1024 sc1\n\t"
        "buffer_load_dwordx4 v[64:67], v2, s[56:59], s36 offen offset:2048 sc1\n\t"
        "buffer_load_dwordx4 v[68:71], v2, s[56:59], s36 offen offset:3072 sc1\n\t"
        "s_waitcnt vmcnt(8)\n\t"
        "v_add_f32_dpp v88, v88, v88 row_ror:8 row_mask:0xf bank_mask:0xf\n\t"
        "v_add_f32_dpp v89, v89, v89 row_ror:8 row_mask:0xf bank_mask:0xf\n\t"
        "v_add_f32_dpp v90, v90, v90 row_ror:8 row_mask:0xf bank_mask:0xf\n\t"
        "v_add_f32_dpp v91, v91, v91 row_ror:8 row_mask:0xf bank_mask:0xf\n\t"
        "v_mov_b32 v6, v88\n\t"
        "v_mov_b32 v7, v89\n\t"
        "v_mov_b32 v8, v90\n\t"
        "v_mov_b32 v9, v91\n\t"
        "v_permlane16_swap_b32 v88, v6\n\t"
        "v_permlane16_swap_b32 v89, v7\n\t"
        "v_permlane16_swap_b32 v90, v8\n\t"
        "v_permlane16_swap_b32 v91, v9\n\t"
        "v_add_f32 v88, v88, v6\n\t"
        "v_add_f32 v89, v89, v7\n\t"
        "v_add_f32 v90, v90, v8\n\t"
        "v_add_f32 v91, v91, v9\n\t"
        "v_mov_b32 v6, v88\n\t"
        "v_mov_b32 v7, v89\n\t"
        "v_mov_b32 v8, v90\n\t"
        "v_mov_b32 v9, v91\n\t"
        "v_permlane32_swap_b32 v88, v6\n\t"
        "v_permlane32_swap_b32 v89, v7\n\t"
        "v_permlane32_swap_b32 v90, v8\n\t"
        "v_permlane32_swap_b32 v91, v9\n\t"
        "v_add_f32 v88, v88, v6\n\t"
        "v_add_f32 v89, v89, v7\n\t"
        "v_add_f32 v90, v90, v8\n\t"
        "v_add_f32 v91, v91, v9\n\t"
        "s_lshl_b32 s33, s24, 7\n\t"
        "s_add_u32 s33, s33, 155648\n\t"
        "v_add_u32 v6, s33, v2\n\t"
        "s_mov_b64 exec, 0xff\n\t"
        "ds_write_b128 v6, v[88:91]\n\t"
        "s_mov_b64 exec, -1\n\t"
        "s_waitcnt vmcnt(7)\n\t"
        "ds_write_b128 v41, v[72:75] offset:16384\n\t"
        "s_waitcnt vmcnt(6)\n\t"
        "ds_write_b128 v41, v[76:79] offset:17408\n\t"
        "s_waitcnt vmcnt(5)\n\t"
        "ds_write_b128 v41, v[80:83] offset:18432\n\t"
        "s_waitcnt vmcnt(4)\n\t"
        "ds_write_b128 v41, v[84:87] offset:19456\n\t"
        "s_add_u32 s36, s7, 49152\n\t"
        "buffer_load_dwordx4 v[72:75], v2, s[56:59], s36 offen offset:0 sc1\n\t"
        "buffer_load_dwordx4 v[76:79], v2, s[56:59], s36 offen offset:1024 sc1\n\t"
        "buffer_load_dwordx4 v[80:83], v2, s[56:59], s36 offen offset:2048 sc1\n\t"
        "buffer_load_dwordx4 v[84:87], v2, s[56:59], s36 offen offset:3072 sc1\n\t"
        "s_waitcnt lgkmcnt(0)\n\t"
        "s_barrier\n\t"
        "ds_read_b128 v[120:123], v42 offset:0\n\t"
        "ds_read_b128 v[124:127], v42 offset:1024\n\t"
        "ds_read_b128 v[128:131], v42 offset:2048\n\t"
        "s_waitcnt lgkmcnt(2)\n\t"
        "v_mfma_f32_16x16x32_bf16 v[96:99], v[160:163], v[120:123], 0\n\t"
        "ds_read_b128 v[132:135], v42 offset:3072\n\t"
        "s_waitcnt vmcnt(7)\n\t"
        "ds_write_b128 v41, v[56:59] offset:32768\n\t"
        "v_mfma_f32_16x16x32_bf16 v[100:103], v[224:227], v[120:123], 0\n\t"
        "ds_read_b32 v142, v43 offset:0\n\t"
        "v_mfma_f32_16x16x32_bf16 v[104:107], a[32:35], v[120:123], 0\n\t"
        "s_waitcnt vmcnt(6)\n\t"
        "ds_write_b128 v41, v[60:63] offset:33792\n\t"
        "s_waitcnt lgkmcnt(5)\n\t"
        "v_mfma_f32_16x16x32_bf16 v[96:99], v[164:167], v[124:127], v[96:99]\n\t"
        "ds_read_b128 v[120:123], v42 offset:4096\n\t"
        "ds_read_b32 v143, v43 offset:64\n\t"
        "v_mfma_f32_16x16x32_bf16 v[100:103], v[228:231], v[124:127], v[100:103]\n\t"
        "s_waitcnt vmcnt(5)\n\t"
        "ds_write_b128 v41, v[64:67] offset:34816\n\t"
        "v_mfma_f32_16x16x32_bf16 v[104:107], a[36:39], v[124:127], v[104:107]\n\t"
        "s_waitcnt lgkmcnt(4)\n\t"
        "v_mul_f32 v142, s52, v142\n\t"
        "v_mfma_f32_16x16x32_bf16 v[96:99], v[168:171], v[128:131], v[96:99]\n\t"
        "ds_read_b128 v[124:127], v42 offset:5120\n\t"
        "s_waitcnt vmcnt(4)\n\t"
        "ds_write_b128 v41, v[68:71] offset:35840\n\t"
        "v_mfma_f32_16x16x32_bf16 v[100:103], v[232:235], v[128:131], v[100:103]\n\t"
        "s_waitcnt lgkmcnt(3)\n\t"
        "v_mul_f32 v143, s52, v143\n\t"
        "v_mfma_f32_16x16x32_bf16 v[104:107], a[40:43], v[128:131], v[104:107]\n\t"
        "v_fma_f32 v143, -v142, v142, v143\n\t"
        "v_mfma_f32_16x16x32_bf16 v[96:99], v[172:175], v[132:135], v[96:99]\n\t"
        "ds_read_b128 v[128:131], v42 offset:6144\n\t"
        "v_mfma_f32_16x16x32_bf16 v[100:103], v[236:239], v[132:135], v[100:103]\n\t"
        "v_max_f32 v143, 0, v143\n\t"
        "v_mfma_f32_16x16x32_bf16 v[104:107], a[44:47], v[132:135], v[104:107]\n\t"
        "v_add_f32 v143, s53, v143\n\t"
        "v_mfma_f32_16x16x32_bf16 v[96:99], v[176:179], v[120:123], v[96:99]\n\t"
        "ds_read_b128 v[132:135], v42 offset:7168\n\t"
        "v_sub_f32 v142, 0, v142\n\t"
        "v_mfma_f32_16x16x32_bf16 v[100:103], v[240:243], v[120:123], v[100:103]\n\t"
        "v_sqrt_f32 v143, v143\n\t"
        "v_mfma_f32_16x16x32_bf16 v[104:107], a[48:51], v[120:123], v[104:107]\n\t"
        "v_cvt_pk_bf16_f32 v88, v142, v142\n\t"
        "s_waitcnt lgkmcnt(3)\n\t"
        "v_mfma_f32_16x16x32_bf16 v[96:99], v[180:183], v[124:127], v[96:99]\n\t"
        "ds_read_b128 v[120:123], v42 offset:8192\n\t"
        "v_lshlrev_b32 v144, 16, v88\n\t"
        "v_mfma_f32_16x16x32_bf16 v[100:103], v[244:247], v[124:127], v[100:103]\n\t"
        "v_cvt_pk_bf16_f32 v90, v143, v143\n\t"
        "v_mfma_f32_16x16x32_bf16 v[104:107], a[52:55], v[124:127], v[104:107]\n\t"
        "v_sub_f32 v144, v142, v144\n\t"
        "s_waitcnt lgkmcnt(2)\n\t"
        "v_mfma_f32_16x16x32_bf16 v[96:99], v[184:187], v[128:131], v[96:99]\n\t"
        "ds_read_b128 v[124:127], v42 offset:9216\n\t"
        "v_lshlrev_b32 v145, 16, v90\n\t"
        "v_mfma_f32_16x16x32_bf16 v[100:103], v[248:251], v[128:131], v[100:103]\n\t"
        "v_cvt_pk_bf16_f32 v89, v144, v143\n\t"
        "v_mfma_f32_16x16x32_bf16 v[104:107], a[56:59], v[128:131], v[104:107]\n\t"
        "v_sub_f32 v145, v143, v145\n\t"
        "s_waitcnt lgkmcnt(2)\n\t"
        "v_mfma_f32_16x16x32_bf16 v[96:99], v[188:191], v[132:135], v[96:99]\n\t"
        "ds_read_b128 v[128:131], v42 offset:10240\n\t"
        "v_mfma_f32_16x16x32_bf16 v[100:103], v[252:255], v[132:135], v[100:103]\n\t"
        "v_mov_b32 v91, 0\n\t"
        "v_mfma_f32_16x16x32_bf16 v[104:107], a[60:63], v[132:135], v[104:107]\n\t"
        "v_cvt_pk_bf16_f32 v90, v143, v145\n\t"
        "s_waitcnt lgkmcnt(0)\n\t"
        "s_barrier\n\t"
        "v_mfma_f32_16x16x32_bf16 v[96:99], v[192:195], v[120:123], v[96:99]\n\t"
        "ds_read_b128 v[132:135], v42 offset:11264\n\t"
        "ds_read_b32 v142, v43 offset:128\n\t"
        "s_lshl_b32 s38, s24, 7\n\t"
        "v_mfma_f32_16x16x32_bf16 v[100:103], a[0:3], v[120:123], v[100:103]\n\t"
        "s_add_u32 s6, s6, 4096\n\t"
        "ds_read_b32 v143, v43 offset:192\n\t"
        "v_mfma_f32_16x16x32_bf16 v[104:107], a[64:67], v[120:123], v[104:107]\n\t"
        "v_add_u32 v6, s38, v43\n\t"
        "buffer_load_dwordx4 a[144:147], v2, s[56:59], s6 offen offset:0 sc1\n\t"
        "v_mfma_f32_16x16x32_bf16 v[96:99], v[196:199], v[124:127], v[96:99]\n\t"
        "ds_read_b128 v[120:123], v42 offset:12288\n\t"
        "s_waitcnt lgkmcnt(2)\n\t"
        "v_mul_f32 v142, s52, v142\n\t"
        "v_mfma_f32_16x16x32_bf16 v[100:103], a[4:7], v[124:127], v[100:103]\n\t"
        "ds_read_b32 v7, v6\n\t"
        "buffer_load_dwordx4 a[148:151], v2, s[56:59], s6 offen offset:1024 sc1\n\t"
        "v_mfma_f32_16x16x32_bf16 v[104:107], a[68:71], v[124:127], v[104:107]\n\t"
        "s_waitcnt lgkmcnt(2)\n\t"
        "v_mul_f32 v143, s52, v143\n\t"
        "ds_read_b32 v8, v6 offset:64\n\t"
        "v_mfma_f32_16x16x32_bf16 v[96:99], v[200:203], v[128:131], v[96:99]\n\t"
        "ds_read_b128 v[124:127], v42 offset:13312\n\t"
        "buffer_load_dwordx4 a[152:155], v2, s[56:59], s6 offen offset:2048 sc1\n\t"
        "v_mfma_f32_16x16x32_bf16 v[100:103], a[8:11], v[128:131], v[100:103]\n\t"
        "v_fma_f32 v143, -v142, v142, v143\n\t"
        "s_waitcnt lgkmcnt(2)\n\t"
        "v_mul_f32 v7, s52, v7\n\t"
        "v_mfma_f32_16x16x32_bf16 v[104:107], a[72:75], v[128:131], v[104:107]\n\t"
        "buffer_load_dwordx4 a[156:159], v2, s[56:59], s6 offen offset:3072 sc1\n\t"
        "v_max_f32 v143, 0, v143\n\t"
        "v_mfma_f32_16x16x32_bf16 v[96:99], v[204:207], v[132:135], v[96:99]\n\t"
        "ds_read_b128 v[128:131], v42 offset:14336\n\t"
        "s_waitcnt lgkmcnt(2)\n\t"
        "v_mul_f32 v8, s52, v8\n\t"
        "v_mfma_f32_16x16x32_bf16 v[100:103], a[12:15], v[132:135], v[100:103]\n\t"
        "s_add_u32 s6, s6, 4096\n\t"
        "v_add_f32 v143, s53, v143\n\t"
        "v_mfma_f32_16x16x32_bf16 v[104:107], a[76:79], v[132:135], v[104:107]\n\t"
        "v_fma_f32 v8, -v7, v7, v8\n\t"
        "buffer_load_dwordx4 a[160:163], v2, s[56:59], s6 offen offset:0 sc1\n\t"
        "v_mfma_f32_16x16x32_bf16 v[96:99], v[208:211], v[120:123], v[96:99]\n\t"
        "ds_read_b128 v[132:135], v42 offset:15360\n\t"
        "v_sub_f32 v142, 0, v142\n\t"
        "v_max_f32 v8, 0, v8\n\t"
        "v_mfma_f32_16x16x32_bf16 v[100:103], a[16:19], v[120:123], v[100:103]\n\t"
        "buffer_load_dwordx4 a[164:167], v2, s[56:59], s6 offen offset:1024 sc1\n\t"
        "v_sqrt_f32 v143, v143\n\t"
        "v_mfma_f32_16x16x32_bf16 v[104:107], a[80:83], v[120:123], v[104:107]\n\t"
        "v_add_f32 v8, s53, v8\n\t"
        "buffer_load_dwordx4 a[168:171], v2, s[56:59], s6 offen offset:2048 sc1\n\t"
        "s_waitcnt lgkmcnt(2)\n\t"
        "v_mfma_f32_16x16x32_bf16 v[96:99], v[212:215], v[124:127], v[96:99]\n\t"
        "ds_read_b128 v[120:123], v42 offset:16384\n\t"
        "v_cvt_pk_bf16_f32 v136, v142, v142\n\t"
        "v_mfma_f32_16x16x32_bf16 v[100:103], a[20:23], v[124:127], v[100:103]\n\t"
        "v_rsq_f32 v94, v8\n\t"
        "buffer_load_dwordx4 a[172:175], v2, s[56:59], s6 offen offset:3072 sc1\n\t"
        "v_mfma_f32_16x16x32_bf16 v[104:107], a[84:87], v[124:127], v[104:107]\n\t"
        "v_lshlrev_b32 v144, 16, v136\n\t"
        "s_add_u32 s6, s6, 4096\n\t"
        "s_waitcnt lgkmcnt(2)\n\t"
        "v_mfma_f32_16x16x32_bf16 v[96:99], v[216:219], v[128:131], v[96:99]\n\t"
        "ds_read_b128 v[124:127], v42 offset:17408\n\t"
        "v_cvt_pk_bf16_f32 v138, v143, v143\n\t"
        "v_mfma_f32_16x16x32_bf16 v[100:103], a[24:27], v[128:131], v[100:103]\n\t"
        "buffer_load_dwordx4 a[176:179], v2, s[56:59], s6 offen offset:0 sc1\n\t"
        "v_sub_f32 v144, v142, v144\n\t"
        "v_mfma_f32_16x16x32_bf16 v[104:107], a[88:91], v[128:131], v[104:107]\n\t"
        "buffer_load_dwordx4 a[180:183], v2, s[56:59], s6 offen offset:1024 sc1\n\t"
        "v_lshlrev_b32 v145, 16, v138\n\t"
        "s_waitcnt lgkmcnt(2)\n\t"
        "v_mfma_f32_16x16x32_bf16 v[96:99], v[220:223], v[132:135], v[96:99]\n\t"
        "ds_read_b128 v[128:131], v42 offset:18432\n\t"
        "buffer_load_dwordx4 a[184:187], v2, s[56:59], s6 offen offset:2048 sc1\n\t"
        "v_mfma_f32_16x16x32_bf16 v[100:103], a[28:31], v[132:135], v[100:103]\n\t"
        "v_cvt_pk_bf16_f32 v137, v144, v143\n\t"
        "v_sub_f32 v145, v143, v145\n\t"
        "v_mfma_f32_16x16x32_bf16 v[104:107], a[92:95], v[132:135], v[104:107]\n\t"
        "v_mov_b32 v139, 0\n\t"
        "v_cvt_pk_bf16_f32 v138, v143, v145\n\t"
        "v_mfma_f32_16x16x32_bf16 v[96:99], v[44:47], v[88:91], v[96:99]\n\t"
        "v_mfma_f32_16x16x32_bf16 v[100:103], v[48:51], v[88:91], v[100:103]\n\t"
        "v_mfma_f32_16x16x32_bf16 v[104:107], v[52:55], v[88:91], v[104:107]\n\t"
        "s_waitcnt lgkmcnt(2)\n\t"
        "v_mfma_f32_16x16x32_bf16 v[108:111], v[160:163], v[120:123], 0\n\t"
        "ds_read_b128 v[132:135], v42 offset:19456\n\t"
        "s_waitcnt vmcnt(14)\n\t"
        "ds_write_b128 v41, v[72:75] offset:49152\n\t"
        "v_mfma_f32_16x16x32_bf16 v[112:115], v[224:227], v[120:123], 0\n\t"
        "s_waitcnt vmcnt(13)\n\t"
        "ds_write_b128 v41, v[76:79] offset:50176\n\t"
        "v_mfma_f32_16x16x32_bf16 v[116:119], a[32:35], v[120:123], 0\n\t"
        "v_cvt_pk_bf16_f32 v148, v96, v97\n\t"
        "s_waitcnt lgkmcnt(4)\n\t"
        "v_mfma_f32_16x16x32_bf16 v[108:111], v[164:167], v[124:127], v[108:111]\n\t"
        "ds_read_b128 v[120:123], v42 offset:20480\n\t"
        "v_mfma_f32_16x16x32_bf16 v[112:115], v[228:231], v[124:127], v[112:115]\n\t"
        "s_waitcnt vmcnt(12)\n\t"
        "ds_write_b128 v41, v[80:83] offset:51200\n\t"
        "v_mfma_f32_16x16x32_bf16 v[116:119], a[36:39], v[124:127], v[116:119]\n\t"
        "v_cvt_pk_bf16_f32 v149, v98, v99\n\t"
        "s_waitcnt lgkmcnt(5)\n\t"
        "v_mfma_f32_16x16x32_bf16 v[108:111], v[168:171], v[128:131], v[108:111]\n\t"
        "ds_read_b128 v[124:127], v42 offset:21504\n\t"
        "s_waitcnt vmcnt(11)\n\t"
        "ds_write_b128 v41, v[84:87] offset:52224\n\t"
        "v_mfma_f32_16x16x32_bf16 v[112:115], v[232:235], v[128:131], v[112:115]\n\t"
        "v_pk_max_i16 v148, v148, 0\n\t"
        "v_mfma_f32_16x16x32_bf16 v[116:119], a[40:43], v[128:131], v[116:119]\n\t"
        "v_pk_max_i16 v149, v149, 0\n\t"
        "s_waitcnt lgkmcnt(6)\n\t"
        "v_mfma_f32_16x16x32_bf16 v[108:111], v[172:175], v[132:135], v[108:111]\n\t"
        "ds_read_b128 v[128:131], v42 offset:22528\n\t"
        "v_mfma_f32_16x16x32_bf16 v[112:115], v[236:239], v[132:135], v[112:115]\n\t"
        "v_cvt_pk_bf16_f32 v150, v100, v101\n\t"
        "v_mfma_f32_16x16x32_bf16 v[116:119], a[44:47], v[132:135], v[116:119]\n\t"
        "v_cvt_pk_bf16_f32 v151, v102, v103\n\t"
        "s_waitcnt lgkmcnt(4)\n\t"
        "v_mfma_f32_16x16x32_bf16 v[108:111], v[176:179], v[120:123], v[108:111]\n\t"
        "ds_read_b128 v[132:135], v42 offset:23552\n\t"
        "v_pk_max_i16 v150, v150, 0\n\t"
        "v_mfma_f32_16x16x32_bf16 v[112:115], v[240:243], v[120:123], v[112:115]\n\t"
        "v_pk_max_i16 v151, v151, 0\n\t"
        "v_mfma_f32_16x16x32_bf16 v[116:119], a[48:51], v[120:123], v[116:119]\n\t"
        "s_add_u32 s37, s19, 0\n\t"
        "s_waitcnt lgkmcnt(3)\n\t"
        "v_mfma_f32_16x16x32_bf16 v[108:111], v[180:183], v[124:127], v[108:111]\n\t"
        "ds_read_b128 v[120:123], v42 offset:24576\n\t"
        "v_mfma_f32_16x16x32_bf16 v[112:115], v[244:247], v[124:127], v[112:115]\n\t"
        "v_permlane16_swap_b32 v148, v150\n\t"
        "v_mfma_f32_16x16x32_bf16 v[116:119], a[52:55], v[124:127], v[116:119]\n\t"
        "v_permlane16_swap_b32 v149, v151\n\t"
        "s_waitcnt lgkmcnt(2)\n\t"
        "v_mfma_f32_16x16x32_bf16 v[108:111], v[184:187], v[128:131], v[108:111]\n\t"
        "ds_read_b128 v[124:127], v42 offset:25600\n\t"
        "buffer_store_dwordx4 v[148:151], v92, s[56:59], s37 offen sc1\n\t"
        "v_mfma_f32_16x16x32_bf16 v[112:115], v[248:251], v[128:131], v[112:115]\n\t"
        "v_cvt_pk_bf16_f32 v152, v104, v105\n\t"
        "v_mfma_f32_16x16x32_bf16 v[116:119], a[56:59], v[128:131], v[116:119]\n\t"
        "v_cvt_pk_bf16_f32 v153, v106, v107\n\t"
        "s_waitcnt lgkmcnt(2)\n\t"
        "v_mfma_f32_16x16x32_bf16 v[108:111], v[188:191], v[132:135], v[108:111]\n\t"
        "ds_read_b128 v[128:131], v42 offset:26624\n\t"
        "v_mfma_f32_16x16x32_bf16 v[112:115], v[252:255], v[132:135], v[112:115]\n\t"
        "v_pk_max_i16 v152, v152, 0\n\t"
        "v_mfma_f32_16x16x32_bf16 v[116:119], a[60:63], v[132:135], v[116:119]\n\t"
        "v_pk_max_i16 v153, v153, 0\n\t"
        "s_waitcnt lgkmcnt(0)\n\t"
        "s_barrier\n\t"
        "v_mfma_f32_16x16x32_bf16 v[108:111], v[192:195], v[120:123], v[108:111]\n\t"
        "ds_read_b128 v[132:135], v42 offset:27648\n\t"
        "ds_read_b32 v142, v43 offset:256\n\t"
        "v_mfma_f32_16x16x32_bf16 v[112:115], a[0:3], v[120:123], v[112:115]\n\t"
        "buffer_load_dwordx4 a[188:191], v2, s[56:59], s6 offen offset:3072 sc1\n\t"
        "ds_read_b32 v143, v43 offset:320\n\t"
        "v_mfma_f32_16x16x32_bf16 v[116:119], a[64:67], v[120:123], v[116:119]\n\t"
        "s_add_u32 s6, s6, 4096\n\t"
        "v_mfma_f32_16x16x32_bf16 v[108:111], v[196:199], v[124:127], v[108:111]\n\t"
        "ds_read_b128 v[120:123], v42 offset:28672\n\t"
        "s_waitcnt lgkmcnt(2)\n\t"
        "v_mul_f32 v142, s52, v142\n\t"
        "v_mfma_f32_16x16x32_bf16 v[112:115], a[4:7], v[124:127], v[112:115]\n\t"
        "buffer_load_dwordx4 a[192:195], v2, s[56:59], s6 offen offset:0 sc1\n\t"
        "s_waitcnt lgkmcnt(1)\n\t"
        "v_mul_f32 v143, s52, v143\n\t"
        "v_mfma_f32_16x16x32_bf16 v[116:119], a[68:71], v[124:127], v[116:119]\n\t"
        "buffer_load_dwordx4 a[196:199], v2, s[56:59], s6 offen offset:1024 sc1\n\t"
        "v_mfma_f32_16x16x32_bf16 v[108:111], v[200:203], v[128:131], v[108:111]\n\t"
        "ds_read_b128 v[124:127], v42 offset:29696\n\t"
        "v_fma_f32 v143, -v142, v142, v143\n\t"
        "v_mfma_f32_16x16x32_bf16 v[112:115], a[8:11], v[128:131], v[112:115]\n\t"
        "buffer_load_dwordx4 a[200:203], v2, s[56:59], s6 offen offset:2048 sc1\n\t"
        "v_max_f32 v143, 0, v143\n\t"
        "v_mfma_f32_16x16x32_bf16 v[116:119], a[72:75], v[128:131], v[116:119]\n\t"
        "buffer_load_dwordx4 a[204:207], v2, s[56:59], s6 offen offset:3072 sc1\n\t"
        "v_mfma_f32_16x16x32_bf16 v[108:111], v[204:207], v[132:135], v[108:111]\n\t"
        "ds_read_b128 v[128:131], v42 offset:30720\n\t"
        "v_add_f32 v143, s53, v143\n\t"
        "v_mfma_f32_16x16x32_bf16 v[112:115], a[12:15], v[132:135], v[112:115]\n\t"
        "s_add_u32 s6, s6, 4096\n\t"
        "v_mfma_f32_16x16x32_bf16 v[116:119], a[76:79], v[132:135], v[116:119]\n\t"
        "v_sub_f32 v142, 0, v142\n\t"
        "buffer_load_dwordx4 a[208:211], v2, s[56:59], s6 offen offset:0 sc1\n\t"
        "s_waitcnt lgkmcnt(2)\n\t"
        "v_mfma_f32_16x16x32_bf16 v[108:111], v[208:211], v[120:123], v[108:111]\n\t"
        "ds_read_b128 v[132:135], v42 offset:31744\n\t"
        "v_sqrt_f32 v143, v143\n\t"
        "v_mfma_f32_16x16x32_bf16 v[112:115], a[16:19], v[120:123], v[112:115]\n\t"
        "buffer_load_dwordx4 a[212:215], v2, s[56:59], s6 offen offset:1024 sc1\n\t"
        "v_mfma_f32_16x16x32_bf16 v[116:119], a[80:83], v[120:123], v[116:119]\n\t"
        "v_cvt_pk_bf16_f32 v88, v142, v142\n\t"
        "buffer_load_dwordx4 a[216:219], v2, s[56:59], s6 offen offset:2048 sc1\n\t"
        "s_waitcnt lgkmcnt(2)\n\t"
        "v_mfma_f32_16x16x32_bf16 v[108:111], v[212:215], v[124:127], v[108:111]\n\t"
        "ds_read_b128 v[120:123], v42 offset:32768\n\t"
        "v_lshlrev_b32 v144, 16, v88\n\t"
        "v_mfma_f32_16x16x32_bf16 v[112:115], a[20:23], v[124:127], v[112:115]\n\t"
        "buffer_load_dwordx4 a[220:223], v2, s[56:59], s6 offen offset:3072 sc1\n\t"
        "v_mfma_f32_16x16x32_bf16 v[116:119], a[84:87], v[124:127], v[116:119]\n\t"
        "v_cvt_pk_bf16_f32 v90, v143, v143\n\t"
        "s_add_u32 s6, s6, 4096\n\t"
        "s_waitcnt lgkmcnt(2)\n\t"
        "v_mfma_f32_16x16x32_bf16 v[108:111], v[216:219], v[128:131], v[108:111]\n\t"
        "ds_read_b128 v[124:127], v42 offset:33792\n\t"
        "v_sub_f32 v144, v142, v144\n\t"
        "v_mfma_f32_16x16x32_bf16 v[112:115], a[24:27], v[128:131], v[112:115]\n\t"
        "buffer_load_dwordx4 a[224:227], v2, s[56:59], s6 offen offset:0 sc1\n\t"
        "v_mfma_f32_16x16x32_bf16 v[116:119], a[88:91], v[128:131], v[116:119]\n\t"
        "v_lshlrev_b32 v145, 16, v90\n\t"
        "v_cvt_pk_bf16_f32 v89, v144, v143\n\t"
        "s_waitcnt lgkmcnt(2)\n\t"
        "v_mfma_f32_16x16x32_bf16 v[108:111], v[220:223], v[132:135], v[108:111]\n\t"
        "ds_read_b128 v[128:131], v42 offset:34816\n\t"
        "v_mfma_f32_16x16x32_bf16 v[112:115], a[28:31], v[132:135], v[112:115]\n\t"
        "v_sub_f32 v145, v143, v145\n\t"
        "v_mov_b32 v91, 0\n\t"
        "v_mfma_f32_16x16x32_bf16 v[116:119], a[92:95], v[132:135], v[116:119]\n\t"
        "v_cvt_pk_bf16_f32 v90, v143, v145\n\t"
        "v_mfma_f32_16x16x32_bf16 v[108:111], v[44:47], v[136:139], v[108:111]\n\t"
        "v_mfma_f32_16x16x32_bf16 v[112:115], v[48:51], v[136:139], v[112:115]\n\t"
        "v_mfma_f32_16x16x32_bf16 v[116:119], v[52:55], v[136:139], v[116:119]\n\t"
        "s_waitcnt lgkmcnt(2)\n\t"
        "v_mfma_f32_16x16x32_bf16 v[96:99], v[160:163], v[120:123], 0\n\t"
        "ds_read_b128 v[132:135], v42 offset:35840\n\t"
        "v_mfma_f32_16x16x32_bf16 v[100:103], v[224:227], v[120:123], 0\n\t"
        "v_mfma_f32_16x16x32_bf16 v[104:107], a[32:35], v[120:123], 0\n\t"
        "s_nop 0\n\t"
        "v_cvt_pk_bf16_f32 v148, v108, v109\n\t"
        "s_waitcnt lgkmcnt(2)\n\t"
        "v_mfma_f32_16x16x32_bf16 v[96:99], v[164:167], v[124:127], v[96:99]\n\t"
        "ds_read_b128 v[120:123], v42 offset:36864\n\t"
        "v_cvt_pk_bf16_f32 v149, v110, v111\n\t"
        "v_pk_max_i16 v148, v148, 0\n\t"
        "v_mfma_f32_16x16x32_bf16 v[100:103], v[228:231], v[124:127], v[100:103]\n\t"
        "v_pk_max_i16 v149, v149, 0\n\t"
        "v_mfma_f32_16x16x32_bf16 v[104:107], a[36:39], v[124:127], v[104:107]\n\t"
        "v_cvt_pk_bf16_f32 v150, v112, v113\n\t"
        "s_waitcnt lgkmcnt(2)\n\t"
        "v_mfma_f32_16x16x32_bf16 v[96:99], v[168:171], v[128:131], v[96:99]\n\t"
        "ds_read_b128 v[124:127], v42 offset:37888\n\t"
        "v_cvt_pk_bf16_f32 v151, v114, v115\n\t"
        "v_mfma_f32_16x16x32_bf16 v[100:103], v[232:235], v[128:131], v[100:103]\n\t"
        "v_pk_max_i16 v150, v150, 0\n\t"
        "v_mfma_f32_16x16x32_bf16 v[104:107], a[40:43], v[128:131], v[104:107]\n\t"
        "v_pk_max_i16 v151, v151, 0\n\t"
        "s_waitcnt lgkmcnt(2)\n\t"
        "v_mfma_f32_16x16x32_bf16 v[96:99], v[172:175], v[132:135], v[96:99]\n\t"
        "ds_read_b128 v[128:131], v42 offset:38912\n\t"
        "v_mfma_f32_16x16x32_bf16 v[100:103], v[236:239], v[132:135], v[100:103]\n\t"
        "s_add_u32 s37, s19, 44032\n\t"
        "v_mfma_f32_16x16x32_bf16 v[104:107], a[44:47], v[132:135], v[104:107]\n\t"
        "v_permlane16_swap_b32 v148, v150\n\t"
        "s_waitcnt lgkmcnt(2)\n\t"
        "v_mfma_f32_16x16x32_bf16 v[96:99], v[176:179], v[120:123], v[96:99]\n\t"
        "ds_read_b128 v[132:135], v42 offset:39936\n\t"
        "v_permlane16_swap_b32 v149, v151\n\t"
        "v_mfma_f32_16x16x32_bf16 v[100:103], v[240:243], v[120:123], v[100:103]\n\t"
        "buffer_store_dwordx4 v[148:151], v92, s[56:59], s37 offen sc1\n\t"
        "v_mfma_f32_16x16x32_bf16 v[104:107], a[48:51], v[120:123], v[104:107]\n\t"
        "v_cvt_pk_bf16_f32 v154, v116, v117\n\t"
        "s_waitcnt lgkmcnt(2)\n\t"
        "v_mfma_f32_16x16x32_bf16 v[96:99], v[180:183], v[124:127], v[96:99]\n\t"
        "ds_read_b128 v[120:123], v42 offset:40960\n\t"
        "v_mfma_f32_16x16x32_bf16 v[100:103], v[244:247], v[124:127], v[100:103]\n\t"
        "v_cvt_pk_bf16_f32 v155, v118, v119\n\t"
        "v_mfma_f32_16x16x32_bf16 v[104:107], a[52:55], v[124:127], v[104:107]\n\t"
        "v_pk_max_i16 v154, v154, 0\n\t"
        "s_waitcnt lgkmcnt(2)\n\t"
        "v_mfma_f32_16x16x32_bf16 v[96:99], v[184:187], v[128:131], v[96:99]\n\t"
        "ds_read_b128 v[124:127], v42 offset:41984\n\t"
        "v_pk_max_i16 v155, v155, 0\n\t"
        "v_mfma_f32_16x16x32_bf16 v[100:103], v[248:251], v[128:131], v[100:103]\n\t"
        "s_add_u32 s37, s19, 0\n\t"
        "v_mfma_f32_16x16x32_bf16 v[104:107], a[56:59], v[128:131], v[104:107]\n\t"
        "v_permlane16_swap_b32 v152, v154\n\t"
        "s_waitcnt lgkmcnt(2)\n\t"
        "v_mfma_f32_16x16x32_bf16 v[96:99], v[188:191], v[132:135], v[96:99]\n\t"
        "ds_read_b128 v[128:131], v42 offset:43008\n\t"
        "v_mfma_f32_16x16x32_bf16 v[100:103], v[252:255], v[132:135], v[100:103]\n\t"
        "v_permlane16_swap_b32 v153, v155\n\t"
        "v_mfma_f32_16x16x32_bf16 v[104:107], a[60:63], v[132:135], v[104:107]\n\t"
        "buffer_store_dwordx4 v[152:155], v93, s[56:59], s37 offen sc1\n\t"
        "s_waitcnt lgkmcnt(2)\n\t"
        "v_mfma_f32_16x16x32_bf16 v[96:99], v[192:195], v[120:123], v[96:99]\n\t"
        "ds_read_b128 v[132:135], v42 offset:44032\n\t"
        "ds_read_b32 v142, v43 offset:384\n\t"
        "v_mfma_f32_16x16x32_bf16 v[100:103], a[0:3], v[120:123], v[100:103]\n\t"
        "buffer_load_dwordx4 a[228:231], v2, s[56:59], s6 offen offset:1024 sc1\n\t"
        "ds_read_b32 v143, v43 offset:448\n\t"
        "v_mfma_f32_16x16x32_bf16 v[104:107], a[64:67], v[120:123], v[104:107]\n\t"
        "buffer_load_dwordx4 a[232:235], v2, s[56:59], s6 offen offset:2048 sc1\n\t"
        "s_waitcnt lgkmcnt(4)\n\t"
        "v_mfma_f32_16x16x32_bf16 v[96:99], v[196:199], v[124:127], v[96:99]\n\t"
        "ds_read_b128 v[120:123], v42 offset:45056\n\t"
        "s_waitcnt lgkmcnt(2)\n\t"
        "v_mul_f32 v142, s52, v142\n\t"
        "v_mfma_f32_16x16x32_bf16 v[100:103], a[4:7], v[124:127], v[100:103]\n\t"
        "buffer_load_dwordx4 a[236:239], v2, s[56:59], s6 offen offset:3072 sc1\n\t"
        "s_waitcnt lgkmcnt(1)\n\t"
        "v_mul_f32 v143, s52, v143\n\t"
        "v_mfma_f32_16x16x32_bf16 v[104:107], a[68:71], v[124:127], v[104:107]\n\t"
        "s_add_u32 s6, s6, 4096\n\t"
        "v_mfma_f32_16x16x32_bf16 v[96:99], v[200:203], v[128:131], v[96:99]\n\t"
        "ds_read_b128 v[124:127], v42 offset:46080\n\t"
        "v_fma_f32 v143, -v142, v142, v143\n\t"
        "v_mfma_f32_16x16x32_bf16 v[100:103], a[8:11], v[128:131], v[100:103]\n\t"
        "buffer_load_dwordx4 a[240:243], v2, s[56:59], s6 offen offset:0 sc1\n\t"
        "v_max_f32 v143, 0, v143\n\t"
        "v_mfma_f32_16x16x32_bf16 v[104:107], a[72:75], v[128:131], v[104:107]\n\t"
        "buffer_load_dwordx4 a[244:247], v2, s[56:59], s6 offen offset:1024 sc1\n\t"
        "v_mfma_f32_16x16x32_bf16 v[96:99], v[204:207], v[132:135], v[96:99]\n\t"
        "ds_read_b128 v[128:131], v42 offset:47104\n\t"
        "v_add_f32 v143, s53, v143\n\t"
        "v_mfma_f32_16x16x32_bf16 v[100:103], a[12:15], v[132:135], v[100:103]\n\t"
        "buffer_load_dwordx4 a[248:251], v2, s[56:59], s6 offen offset:2048 sc1\n\t"
        "v_mfma_f32_16x16x32_bf16 v[104:107], a[76:79], v[132:135], v[104:107]\n\t"
        "v_sub_f32 v142, 0, v142\n\t"
        "buffer_load_dwordx4 a[252:255], v2, s[56:59], s6 offen offset:3072 sc1\n\t"
        "s_waitcnt lgkmcnt(2)\n\t"
        "v_mfma_f32_16x16x32_bf16 v[96:99], v[208:211], v[120:123], v[96:99]\n\t"
        "ds_read_b128 v[132:135], v42 offset:48128\n\t"
        "v_sqrt_f32 v143, v143\n\t"
        "v_mfma_f32_16x16x32_bf16 v[100:103], a[16:19], v[120:123], v[100:103]\n\t"
        "s_add_u32 s6, s6, 4096\n\t"
        "v_mfma_f32_16x16x32_bf16 v[104:107], a[80:83], v[120:123], v[104:107]\n\t"
        "v_cvt_pk_bf16_f32 v136, v142, v142\n\t"
        "buffer_load_dwordx4 v[24:27], v40, s[56:59], s6 offen offset:0 sc1\n\t"
        "s_waitcnt lgkmcnt(2)\n\t"
        "v_mfma_f32_16x16x32_bf16 v[96:99], v[212:215], v[124:127], v[96:99]\n\t"
        "ds_read_b128 v[120:123], v42 offset:49152\n\t"
        "v_lshlrev_b32 v144, 16, v136\n\t"
        "v_mfma_f32_16x16x32_bf16 v[100:103], a[20:23], v[124:127], v[100:103]\n\t"
        "buffer_load_dwordx4 v[28:31], v40, s[56:59], s6 offen offset:1024 sc1\n\t"
        "v_mfma_f32_16x16x32_bf16 v[104:107], a[84:87], v[124:127], v[104:107]\n\t"
        "v_cvt_pk_bf16_f32 v138, v143, v143\n\t"
        "buffer_load_dwordx4 v[32:35], v40, s[56:59], s6 offen offset:2048 sc1\n\t"
        "s_waitcnt lgkmcnt(2)\n\t"
        "v_mfma_f32_16x16x32_bf16 v[96:99], v[216:219], v[128:131], v[96:99]\n\t"
        "ds_read_b128 v[124:127], v42 offset:50176\n\t"
        "v_sub_f32 v144, v142, v144\n\t"
        "v_mfma_f32_16x16x32_bf16 v[100:103], a[24:27], v[128:131], v[100:103]\n\t"
        "buffer_load_dwordx4 v[36:39], v40, s[56:59], s6 offen offset:3072 sc1\n\t"
        "v_mfma_f32_16x16x32_bf16 v[104:107], a[88:91], v[128:131], v[104:107]\n\t"
        "v_lshlrev_b32 v145, 16, v138\n\t"
        "v_cvt_pk_bf16_f32 v137, v144, v143\n\t"
        "s_waitcnt lgkmcnt(2)\n\t"
        "v_mfma_f32_16x16x32_bf16 v[96:99], v[220:223], v[132:135], v[96:99]\n\t"
        "ds_read_b128 v[128:131], v42 offset:51200\n\t"
        "v_mfma_f32_16x16x32_bf16 v[100:103], a[28:31], v[132:135], v[100:103]\n\t"
        "v_sub_f32 v145, v143, v145\n\t"
        "v_mov_b32 v139, 0\n\t"
        "v_mfma_f32_16x16x32_bf16 v[104:107], a[92:95], v[132:135], v[104:107]\n\t"
        "v_cvt_pk_bf16_f32 v138, v143, v145\n\t"
        "v_mfma_f32_16x16x32_bf16 v[96:99], v[44:47], v[88:91], v[96:99]\n\t"
        "v_mfma_f32_16x16x32_bf16 v[100:103], v[48:51], v[88:91], v[100:103]\n\t"
        "v_mfma_f32_16x16x32_bf16 v[104:107], v[52:55], v[88:91], v[104:107]\n\t"
        "s_waitcnt lgkmcnt(2)\n\t"
        "v_mfma_f32_16x16x32_bf16 v[108:111], v[160:163], v[120:123], 0\n\t"
        "ds_read_b128 v[132:135], v42 offset:52224\n\t"
        "v_mfma_f32_16x16x32_bf16 v[112:115], v[224:227], v[120:123], 0\n\t"
        "v_mfma_f32_16x16x32_bf16 v[116:119], a[32:35], v[120:123], 0\n\t"
        "s_waitcnt lgkmcnt(2)\n\t"
        "v_mfma_f32_16x16x32_bf16 v[108:111], v[164:167], v[124:127], v[108:111]\n\t"
        "ds_read_b128 v[120:123], v42 offset:53248\n\t"
        "v_cvt_pk_bf16_f32 v148, v96, v97\n\t"
        "v_cvt_pk_bf16_f32 v149, v98, v99\n\t"
        "v_pk_max_i16 v148, v148, 0\n\t"
        "v_mfma_f32_16x16x32_bf16 v[112:115], v[228:231], v[124:127], v[112:115]\n\t"
        "v_pk_max_i16 v149, v149, 0\n\t"
        "v_mfma_f32_16x16x32_bf16 v[116:119], a[36:39], v[124:127], v[116:119]\n\t"
        "s_waitcnt lgkmcnt(2)\n\t"
        "v_mfma_f32_16x16x32_bf16 v[108:111], v[168:171], v[128:131], v[108:111]\n\t"
        "ds_read_b128 v[124:127], v42 offset:54272\n\t"
        "v_cvt_pk_bf16_f32 v150, v100, v101\n\t"
        "v_mfma_f32_16x16x32_bf16 v[112:115], v[232:235], v[128:131], v[112:115]\n\t"
        "v_cvt_pk_bf16_f32 v151, v102, v103\n\t"
        "v_mfma_f32_16x16x32_bf16 v[116:119], a[40:43], v[128:131], v[116:119]\n\t"
        "s_waitcnt lgkmcnt(2)\n\t"
        "v_mfma_f32_16x16x32_bf16 v[108:111], v[172:175], v[132:135], v[108:111]\n\t"
        "ds_read_b128 v[128:131], v42 offset:55296\n\t"
        "v_pk_max_i16 v150, v150, 0\n\t"
        "v_mfma_f32_16x16x32_bf16 v[112:115], v[236:239], v[132:135], v[112:115]\n\t"
        "v_pk_max_i16 v151, v151, 0\n\t"
        "v_mfma_f32_16x16x32_bf16 v[116:119], a[44:47], v[132:135], v[116:119]\n\t"
        "s_waitcnt lgkmcnt(2)\n\t"
        "v_mfma_f32_16x16x32_bf16 v[108:111], v[176:179], v[120:123], v[108:111]\n\t"
        "ds_read_b128 v[132:135], v42 offset:56320\n\t"
        "s_add_u32 s37, s19, 88064\n\t"
        "v_mfma_f32_16x16x32_bf16 v[112:115], v[240:243], v[120:123], v[112:115]\n\t"
        "v_permlane16_swap_b32 v148, v150\n\t"
        "v_mfma_f32_16x16x32_bf16 v[116:119], a[48:51], v[120:123], v[116:119]\n\t"
        "s_waitcnt lgkmcnt(2)\n\t"
        "v_mfma_f32_16x16x32_bf16 v[108:111], v[180:183], v[124:127], v[108:111]\n\t"
        "ds_read_b128 v[120:123], v42 offset:57344\n\t"
        "v_permlane16_swap_b32 v149, v151\n\t"
        "v_mfma_f32_16x16x32_bf16 v[112:115], v[244:247], v[124:127], v[112:115]\n\t"
        "buffer_store_dwordx4 v[148:151], v92, s[56:59], s37 offen sc1\n\t"
        "v_mfma_f32_16x16x32_bf16 v[116:119], a[52:55], v[124:127], v[116:119]\n\t"
        "s_waitcnt lgkmcnt(2)\n\t"
        "v_mfma_f32_16x16x32_bf16 v[108:111], v[184:187], v[128:131], v[108:111]\n\t"
        "ds_read_b128 v[124:127], v42 offset:58368\n\t"
        "v_cvt_pk_bf16_f32 v152, v104, v105\n\t"
        "v_mfma_f32_16x16x32_bf16 v[112:115], v[248:251], v[128:131], v[112:115]\n\t"
        "v_cvt_pk_bf16_f32 v153, v106, v107\n\t"
        "v_mfma_f32_16x16x32_bf16 v[116:119], a[56:59], v[128:131], v[116:119]\n\t"
        "s_waitcnt lgkmcnt(2)\n\t"
        "v_mfma_f32_16x16x32_bf16 v[108:111], v[188:191], v[132:135], v[108:111]\n\t"
        "ds_read_b128 v[128:131], v42 offset:59392\n\t"
        "v_pk_max_i16 v152, v152, 0\n\t"
        "v_mfma_f32_16x16x32_bf16 v[112:115], v[252:255], v[132:135], v[112:115]\n\t"
        "v_pk_max_i16 v153, v153, 0\n\t"
        "v_mfma_f32_16x16x32_bf16 v[116:119], a[60:63], v[132:135], v[116:119]\n\t"
        "s_waitcnt lgkmcnt(2)\n\t"
        "v_mfma_f32_16x16x32_bf16 v[108:111], v[192:195], v[120:123], v[108:111]\n\t"
        "ds_read_b128 v[132:135], v42 offset:60416\n\t"
        "v_mfma_f32_16x16x32_bf16 v[112:115], a[0:3], v[120:123], v[112:115]\n\t"
        "v_mfma_f32_16x16x32_bf16 v[116:119], a[64:67], v[120:123], v[116:119]\n\t"
        "s_waitcnt lgkmcnt(2)\n\t"
        "v_mfma_f32_16x16x32_bf16 v[108:111], v[196:199], v[124:127], v[108:111]\n\t"
        "ds_read_b128 v[120:123], v42 offset:61440\n\t"
        "v_mfma_f32_16x16x32_bf16 v[112:115], a[4:7], v[124:127], v[112:115]\n\t"
        "v_mfma_f32_16x16x32_bf16 v[116:119], a[68:71], v[124:127], v[116:119]\n\t"
        "s_waitcnt lgkmcnt(2)\n\t"
        "v_mfma_f32_16x16x32_bf16 v[108:111], v[200:203], v[128:131], v[108:111]\n\t"
        "ds_read_b128 v[124:127], v42 offset:62464\n\t"
        "v_mfma_f32_16x16x32_bf16 v[112:115], a[8:11], v[128:131], v[112:115]\n\t"
        "v_mfma_f32_16x16x32_bf16 v[116:119], a[72:75], v[128:131], v[116:119]\n\t"
        "s_waitcnt lgkmcnt(2)\n\t"
        "v_mfma_f32_16x16x32_bf16 v[108:111], v[204:207], v[132:135], v[108:111]\n\t"
        "ds_read_b128 v[128:131], v42 offset:63488\n\t"
        "v_mfma_f32_16x16x32_bf16 v[112:115], a[12:15], v[132:135], v[112:115]\n\t"
        "v_mfma_f32_16x16x32_bf16 v[116:119], a[76:79], v[132:135], v[116:119]\n\t"
        "s_waitcnt lgkmcnt(2)\n\t"
        "v_mfma_f32_16x16x32_bf16 v[108:111], v[208:211], v[120:123], v[108:111]\n\t"
        "ds_read_b128 v[132:135], v42 offset:64512\n\t"
        "v_mfma_f32_16x16x32_bf16 v[112:115], a[16:19], v[120:123], v[112:115]\n\t"
        "v_mfma_f32_16x16x32_bf16 v[116:119], a[80:83], v[120:123], v[116:119]\n\t"
        "s_waitcnt lgkmcnt(2)\n\t"
        "v_mfma_f32_16x16x32_bf16 v[108:111], v[212:215], v[124:127], v[108:111]\n\t"
        "v_mfma_f32_16x16x32_bf16 v[112:115], a[20:23], v[124:127], v[112:115]\n\t"
        "v_mfma_f32_16x16x32_bf16 v[116:119], a[84:87], v[124:127], v[116:119]\n\t"
        "s_waitcnt lgkmcnt(1)\n\t"
        "v_mfma_f32_16x16x32_bf16 v[108:111], v[216:219], v[128:131], v[108:111]\n\t"
        "v_mfma_f32_16x16x32_bf16 v[112:115], a[24:27], v[128:131], v[112:115]\n\t"
        "v_mfma_f32_16x16x32_bf16 v[116:119], a[88:91], v[128:131], v[116:119]\n\t"
        "s_waitcnt lgkmcnt(0)\n\t"
        "v_mfma_f32_16x16x32_bf16 v[108:111], v[220:223], v[132:135], v[108:111]\n\t"
        "v_mfma_f32_16x16x32_bf16 v[112:115], a[28:31], v[132:135], v[112:115]\n\t"
        "v_mfma_f32_16x16x32_bf16 v[116:119], a[92:95], v[132:135], v[116:119]\n\t"
        "v_mfma_f32_16x16x32_bf16 v[108:111], v[44:47], v[136:139], v[108:111]\n\t"
        "v_mfma_f32_16x16x32_bf16 v[112:115], v[48:51], v[136:139], v[112:115]\n\t"
        "v_mfma_f32_16x16x32_bf16 v[116:119], v[52:55], v[136:139], v[116:119]\n\t"
        "s_nop 5\n\t"
        "v_cvt_pk_bf16_f32 v148, v108, v109\n\t"
        "v_cvt_pk_bf16_f32 v149, v110, v111\n\t"
        "v_pk_max_i16 v148, v148, 0\n\t"
        "v_pk_max_i16 v149, v149, 0\n\t"
        "v_cvt_pk_bf16_f32 v150, v112, v113\n\t"
        "v_cvt_pk_bf16_f32 v151, v114, v115\n\t"
        "v_pk_max_i16 v150, v150, 0\n\t"
        "v_pk_max_i16 v151, v151, 0\n\t"
        "s_add_u32 s37, s19, 132096\n\t"
        "v_permlane16_swap_b32 v148, v150\n\t"
        "v_permlane16_swap_b32 v149, v151\n\t"
        "buffer_store_dwordx4 v[148:151], v92, s[56:59], s37 offen sc1\n\t"
        "v_cvt_pk_bf16_f32 v154, v116, v117\n\t"
        "v_cvt_pk_bf16_f32 v155, v118, v119\n\t"
        "v_pk_max_i16 v154, v154, 0\n\t"
        "v_pk_max_i16 v155, v155, 0\n\t"
        "s_add_u32 s37, s19, 88064\n\t"
        "v_permlane16_swap_b32 v152, v154\n\t"
        "v_permlane16_swap_b32 v153, v155\n\t"
        "buffer_store_dwordx4 v[152:155], v93, s[56:59], s37 offen sc1\n\t"
        "s_add_u32 s31, s24, 1\n\t"
        "s_and_b32 s31, s31, 3\n\t"
        "s_mul_i32 s31, s31, 43\n\t"
        "s_mul_i32 s33, s24, 11\n\t"
        "s_add_u32 s31, s31, s33\n\t"
        "s_lshl_b32 s31, s31, 10\n\t"
        "s_add_u32 s20, s31, s19\n\t"
        "s_add_u32 s31, s24, 2\n\t"
        "s_and_b32 s31, s31, 3\n\t"
        "s_mul_i32 s31, s31, 43\n\t"
        "s_mul_i32 s33, s24, 11\n\t"
        "s_add_u32 s31, s31, s33\n\t"
        "s_lshl_b32 s31, s31, 10\n\t"
        "s_add_u32 s21, s31, s19\n\t"
        "s_add_u32 s31, s24, 3\n\t"
        "s_and_b32 s31, s31, 3\n\t"
        "s_mul_i32 s31, s31, 43\n\t"
        "s_mul_i32 s33, s24, 11\n\t"
        "s_add_u32 s31, s31, s33\n\t"
        "s_lshl_b32 s31, s31, 10\n\t"
        "s_add_u32 s22, s31, s19\n\t"
        "s_add_u32 s31, s24, 4\n\t"
        "s_and_b32 s31, s31, 3\n\t"
        "s_mul_i32 s31, s31, 43\n\t"
        "s_mul_i32 s33, s24, 11\n\t"
        "s_add_u32 s31, s31, s33\n\t"
        "s_lshl_b32 s31, s31, 10\n\t"
        "s_add_u32 s23, s31, s19\n\t"
        "s_waitcnt vmcnt(0)\n\t"
        "s_barrier\n\t"
        "s_cmp_lg_u32 s24, 0\n\t"
        "s_cbranch_scc1 .Lpub21\n\t"
        "s_add_u32 s31, s5, 8192\n\t"
        "v_mov_b32 v6, 0\n\t"
        "s_mov_b64 exec, 1\n\t"
        "buffer_store_dwordx2 v[14:15], v6, s[56:59], s31 offen sc1\n\t"
        "s_mov_b64 exec, -1\n\t"
        ".Lpub21:\n\t"
        "v_add_u32 v6, 155136, v140\n\t"
        "ds_read_b128 v[144:147], v6\n\t"
        "ds_read_b128 v[148:151], v6 offset:16\n\t"
        "v_add_u32 v7, 155264, v140\n\t"
        "ds_read_b128 v[152:155], v7\n\t"
        "ds_read_b128 v[156:159], v7 offset:16\n\t"
        "s_waitcnt lgkmcnt(3)\n\t"
        "v_pk_mul_f32 v[144:145], v[144:145], v[94:95] op_sel_hi:[1,0]\n\t"
        "v_pk_mul_f32 v[146:147], v[146:147], v[94:95] op_sel_hi:[1,0]\n\t"
        "s_waitcnt lgkmcnt(2)\n\t"
        "v_pk_mul_f32 v[148:149], v[148:149], v[94:95] op_sel_hi:[1,0]\n\t"
        "v_pk_mul_f32 v[150:151], v[150:151], v[94:95] op_sel_hi:[1,0]\n\t"
        "s_waitcnt lgkmcnt(1)\n\t"
        "v_pk_mul_f32 v[152:153], v[152:153], v[94:95] op_sel_hi:[1,0]\n\t"
        "v_pk_mul_f32 v[154:155], v[154:155], v[94:95] op_sel_hi:[1,0]\n\t"
        "s_waitcnt lgkmcnt(0)\n\t"
        "v_pk_mul_f32 v[156:157], v[156:157], v[94:95] op_sel_hi:[1,0]\n\t"
        "v_pk_mul_f32 v[158:159], v[158:159], v[94:95] op_sel_hi:[1,0]\n\t"
        "s_cmp_lg_u32 s24, 0\n\t"
        "s_cbranch_scc1 .Lwf22_done\n\t"
        "v_and_b32 v6, 7, v1\n\t"
        "v_lshlrev_b32 v6, 3, v6\n\t"
        "s_add_u32 s33, s4, 8192\n\t"
        "s_mov_b32 s31, 65536\n\t"
        ".Lwf22_spin:\n\t"
        "buffer_load_dwordx2 v[8:9], v6, s[56:59], s33 offen sc1\n\t"
        "s_waitcnt vmcnt(0)\n\t"
        "v_cmp_ne_u64 vcc, s[34:35], v[8:9]\n\t"
        "s_nop 0\n\t"
        "s_cbranch_vccz .Lwf22_done\n\t"
        "s_sub_u32 s31, s31, 1\n\t"
        "s_cmp_lg_u32 s31, 0\n\t"
        "s_cbranch_scc1 .Lwf22_spin\n\t"
        ".Lwf22_done:\n\t"
        "s_barrier\n\t"
        "buffer_load_dwordx4 v[44:47], v2, s[56:59], s20 offen offset:0 sc1\n\t"
        "buffer_load_dwordx4 v[48:51], v2, s[56:59], s20 offen offset:1024 sc1\n\t"
        "buffer_load_dwordx4 v[52:55], v2, s[56:59], s20 offen offset:2048 sc1\n\t"
        "buffer_load_dwordx4 v[56:59], v2, s[56:59], s20 offen offset:3072 sc1\n\t"
        "s_add_u32 s20, s20, 4096\n\t"
        "buffer_load_dwordx4 v[60:63], v2, s[56:59], s20 offen offset:0 sc1\n\t"
        "buffer_load_dwordx4 v[64:67], v2, s[56:59], s20 offen offset:1024 sc1\n\t"
        "buffer_load_dwordx4 v[68:71], v2, s[56:59], s20 offen offset:2048 sc1\n\t"
        "buffer_load_dwordx4 v[72:75], v2, s[56:59], s20 offen offset:3072 sc1\n\t"
        "s_add_u32 s20, s20, 4096\n\t"
        "buffer_load_dwordx4 v[76:79], v2, s[56:59], s20 offen offset:0 sc1\n\t"
        "buffer_load_dwordx4 v[80:83], v2, s[56:59], s20 offen offset:1024 sc1\n\t"
        "buffer_load_dwordx4 v[84:87], v40, s[56:59], s20 offen offset:2048 sc1\n\t"
        "s_waitcnt vmcnt(10)\n\t"
        "v_mfma_f32_16x16x32_bf16 v[96:99], a[96:99], v[44:47], 0\n\t"
        "v_mfma_f32_16x16x32_bf16 v[100:103], a[100:103], v[44:47], 0\n\t"
        "v_mfma_f32_16x16x32_bf16 v[104:107], a[104:107], v[44:47], 0\n\t"
        "v_mfma_f32_16x16x32_bf16 v[108:111], a[108:111], v[44:47], 0\n\t"
        "buffer_load_dwordx4 v[88:91], v2, s[56:59], s21 offen offset:0 sc1\n\t"
        "s_waitcnt vmcnt(10)\n\t"
        "v_mfma_f32_16x16x32_bf16 v[96:99], a[112:115], v[48:51], v[96:99]\n\t"
        "v_mfma_f32_16x16x32_bf16 v[100:103], a[116:119], v[48:51], v[100:103]\n\t"
        "v_mfma_f32_16x16x32_bf16 v[104:107], a[120:123], v[48:51], v[104:107]\n\t"
        "v_mfma_f32_16x16x32_bf16 v[108:111], a[124:127], v[48:51], v[108:111]\n\t"
        "buffer_load_dwordx4 v[44:47], v2, s[56:59], s21 offen offset:1024 sc1\n\t"
        "s_waitcnt vmcnt(10)\n\t"
        "v_mfma_f32_16x16x32_bf16 v[96:99], a[128:131], v[52:55], v[96:99]\n\t"
        "v_mfma_f32_16x16x32_bf16 v[100:103], a[132:135], v[52:55], v[100:103]\n\t"
        "v_mfma_f32_16x16x32_bf16 v[104:107], a[136:139], v[52:55], v[104:107]\n\t"
        "v_mfma_f32_16x16x32_bf16 v[108:111], a[140:143], v[52:55], v[108:111]\n\t"
        "buffer_load_dwordx4 v[48:51], v2, s[56:59], s21 offen offset:2048 sc1\n\t"
        "s_waitcnt vmcnt(10)\n\t"
        "v_mfma_f32_16x16x32_bf16 v[96:99], a[144:147], v[56:59], v[96:99]\n\t"
        "v_mfma_f32_16x16x32_bf16 v[100:103], a[148:151], v[56:59], v[100:103]\n\t"
        "v_mfma_f32_16x16x32_bf16 v[104:107], a[152:155], v[56:59], v[104:107]\n\t"
        "v_mfma_f32_16x16x32_bf16 v[108:111], a[156:159], v[56:59], v[108:111]\n\t"
        "buffer_load_dwordx4 v[52:55], v2, s[56:59], s21 offen offset:3072 sc1\n\t"
        "s_waitcnt vmcnt(10)\n\t"
        "v_mfma_f32_16x16x32_bf16 v[96:99], a[160:163], v[60:63], v[96:99]\n\t"
        "v_mfma_f32_16x16x32_bf16 v[100:103], a[164:167], v[60:63], v[100:103]\n\t"
        "v_mfma_f32_16x16x32_bf16 v[104:107], a[168:171], v[60:63], v[104:107]\n\t"
        "v_mfma_f32_16x16x32_bf16 v[108:111], a[172:175], v[60:63], v[108:111]\n\t"
        "s_add_u32 s21, s21, 4096\n\t"
        "buffer_load_dwordx4 v[56:59], v2, s[56:59], s21 offen offset:0 sc1\n\t"
        "s_waitcnt vmcnt(10)\n\t"
        "v_mfma_f32_16x16x32_bf16 v[96:99], a[176:179], v[64:67], v[96:99]\n\t"
        "v_mfma_f32_16x16x32_bf16 v[100:103], a[180:183], v[64:67], v[100:103]\n\t"
        "v_mfma_f32_16x16x32_bf16 v[104:107], a[184:187], v[64:67], v[104:107]\n\t"
        "v_mfma_f32_16x16x32_bf16 v[108:111], a[188:191], v[64:67], v[108:111]\n\t"
        "buffer_load_dwordx4 v[60:63], v2, s[56:59], s21 offen offset:1024 sc1\n\t"
        "s_waitcnt vmcnt(10)\n\t"
        "v_mfma_f32_16x16x32_bf16 v[96:99], a[192:195], v[68:71], v[96:99]\n\t"
        "v_mfma_f32_16x16x32_bf16 v[100:103], a[196:199], v[68:71], v[100:103]\n\t"
        "v_mfma_f32_16x16x32_bf16 v[104:107], a[200:203], v[68:71], v[104:107]\n\t"
        "v_mfma_f32_16x16x32_bf16 v[108:111], a[204:207], v[68:71], v[108:111]\n\t"
        "buffer_load_dwordx4 v[64:67], v2, s[56:59], s21 offen offset:2048 sc1\n\t"
        "s_waitcnt vmcnt(10)\n\t"
        "v_mfma_f32_16x16x32_bf16 v[96:99], a[208:211], v[72:75], v[96:99]\n\t"
        "v_mfma_f32_16x16x32_bf16 v[100:103], a[212:215], v[72:75], v[100:103]\n\t"
        "v_mfma_f32_16x16x32_bf16 v[104:107], a[216:219], v[72:75], v[104:107]\n\t"
        "v_mfma_f32_16x16x32_bf16 v[108:111], a[220:223], v[72:75], v[108:111]\n\t"
        "buffer_load_dwordx4 v[68:71], v2, s[56:59], s21 offen offset:3072 sc1\n\t"
        "s_waitcnt vmcnt(10)\n\t"
        "v_mfma_f32_16x16x32_bf16 v[96:99], a[224:227], v[76:79], v[96:99]\n\t"
        "v_mfma_f32_16x16x32_bf16 v[100:103], a[228:231], v[76:79], v[100:103]\n\t"
        "v_mfma_f32_16x16x32_bf16 v[104:107], a[232:235], v[76:79], v[104:107]\n\t"
        "v_mfma_f32_16x16x32_bf16 v[108:111], a[236:239], v[76:79], v[108:111]\n\t"
        "s_add_u32 s21, s21, 4096\n\t"
        "buffer_load_dwordx4 v[72:75], v2, s[56:59], s21 offen offset:0 sc1\n\t"
        "s_waitcnt vmcnt(10)\n\t"
        "v_mfma_f32_16x16x32_bf16 v[96:99], a[240:243], v[80:83], v[96:99]\n\t"
        "v_mfma_f32_16x16x32_bf16 v[100:103], a[244:247], v[80:83], v[100:103]\n\t"
        "v_mfma_f32_16x16x32_bf16 v[104:107], a[248:251], v[80:83], v[104:107]\n\t"
        "v_mfma_f32_16x16x32_bf16 v[108:111], a[252:255], v[80:83], v[108:111]\n\t"
        "buffer_load_dwordx4 v[76:79], v2, s[56:59], s21 offen offset:1024 sc1\n\t"
        "s_waitcnt vmcnt(10)\n\t"
        "v_mfma_f32_16x16x32_bf16 v[96:99], v[24:27], v[84:87], v[96:99]\n\t"
        "v_mfma_f32_16x16x32_bf16 v[100:103], v[28:31], v[84:87], v[100:103]\n\t"
        "v_mfma_f32_16x16x32_bf16 v[104:107], v[32:35], v[84:87], v[104:107]\n\t"
        "v_mfma_f32_16x16x32_bf16 v[108:111], v[36:39], v[84:87], v[108:111]\n\t"
        "buffer_load_dwordx4 v[80:83], v40, s[56:59], s21 offen offset:2048 sc1\n\t"
        "s_waitcnt vmcnt(10)\n\t"
        "v_mfma_f32_16x16x32_bf16 v[112:115], a[96:99], v[88:91], 0\n\t"
        "v_mfma_f32_16x16x32_bf16 v[116:119], a[100:103], v[88:91], 0\n\t"
        "v_mfma_f32_16x16x32_bf16 v[120:123], a[104:107], v[88:91], 0\n\t"
        "v_mfma_f32_16x16x32_bf16 v[124:127], a[108:111], v[88:91], 0\n\t"
        "buffer_load_dwordx4 v[84:87], v2, s[56:59], s22 offen offset:0 sc1\n\t"
        "s_waitcnt vmcnt(10)\n\t"
        "v_mfma_f32_16x16x32_bf16 v[112:115], a[112:115], v[44:47], v[112:115]\n\t"
        "v_mfma_f32_16x16x32_bf16 v[116:119], a[116:119], v[44:47], v[116:119]\n\t"
        "v_mfma_f32_16x16x32_bf16 v[120:123], a[120:123], v[44:47], v[120:123]\n\t"
        "v_mfma_f32_16x16x32_bf16 v[124:127], a[124:127], v[44:47], v[124:127]\n\t"
        "buffer_load_dwordx4 v[88:91], v2, s[56:59], s22 offen offset:1024 sc1\n\t"
        "s_waitcnt vmcnt(10)\n\t"
        "v_mfma_f32_16x16x32_bf16 v[112:115], a[128:131], v[48:51], v[112:115]\n\t"
        "ds_write_b128 v10, v[96:99] offset:0\n\t"
        "v_mfma_f32_16x16x32_bf16 v[116:119], a[132:135], v[48:51], v[116:119]\n\t"
        "ds_write_b128 v10, v[100:103] offset:1024\n\t"
        "v_mfma_f32_16x16x32_bf16 v[120:123], a[136:139], v[48:51], v[120:123]\n\t"
        "ds_write_b128 v10, v[104:107] offset:2048\n\t"
        "v_mfma_f32_16x16x32_bf16 v[124:127], a[140:143], v[48:51], v[124:127]\n\t"
        "ds_write_b128 v10, v[108:111] offset:3072\n\t"
        "buffer_load_dwordx4 v[44:47], v2, s[56:59], s22 offen offset:2048 sc1\n\t"
        "s_waitcnt vmcnt(10)\n\t"
        "v_mfma_f32_16x16x32_bf16 v[112:115], a[144:147], v[52:55], v[112:115]\n\t"
        "v_mfma_f32_16x16x32_bf16 v[116:119], a[148:151], v[52:55], v[116:119]\n\t"
        "v_mfma_f32_16x16x32_bf16 v[120:123], a[152:155], v[52:55], v[120:123]\n\t"
        "v_mfma_f32_16x16x32_bf16 v[124:127], a[156:159], v[52:55], v[124:127]\n\t"
        "buffer_load_dwordx4 v[48:51], v2, s[56:59], s22 offen offset:3072 sc1\n\t"
        "s_waitcnt vmcnt(10)\n\t"
        "v_mfma_f32_16x16x32_bf16 v[112:115], a[160:163], v[56:59], v[112:115]\n\t"
        "v_mfma_f32_16x16x32_bf16 v[116:119], a[164:167], v[56:59], v[116:119]\n\t"
        "v_mfma_f32_16x16x32_bf16 v[120:123], a[168:171], v[56:59], v[120:123]\n\t"
        "v_mfma_f32_16x16x32_bf16 v[124:127], a[172:175], v[56:59], v[124:127]\n\t"
        "s_add_u32 s22, s22, 4096\n\t"
        "buffer_load_dwordx4 v[52:55], v2, s[56:59], s22 offen offset:0 sc1\n\t"
        "s_waitcnt vmcnt(10)\n\t"
        "v_mfma_f32_16x16x32_bf16 v[112:115], a[176:179], v[60:63], v[112:115]\n\t"
        "v_mfma_f32_16x16x32_bf16 v[116:119], a[180:183], v[60:63], v[116:119]\n\t"
        "v_mfma_f32_16x16x32_bf16 v[120:123], a[184:187], v[60:63], v[120:123]\n\t"
        "v_mfma_f32_16x16x32_bf16 v[124:127], a[188:191], v[60:63], v[124:127]\n\t"
        "buffer_load_dwordx4 v[56:59], v2, s[56:59], s22 offen offset:1024 sc1\n\t"
        "s_waitcnt vmcnt(10)\n\t"
        "v_mfma_f32_16x16x32_bf16 v[112:115], a[192:195], v[64:67], v[112:115]\n\t"
        "v_mfma_f32_16x16x32_bf16 v[116:119], a[196:199], v[64:67], v[116:119]\n\t"
        "v_mfma_f32_16x16x32_bf16 v[120:123], a[200:203], v[64:67], v[120:123]\n\t"
        "v_mfma_f32_16x16x32_bf16 v[124:127], a[204:207], v[64:67], v[124:127]\n\t"
        "buffer_load_dwordx4 v[60:63], v2, s[56:59], s22 offen offset:2048 sc1\n\t"
        "s_waitcnt vmcnt(10)\n\t"
        "v_mfma_f32_16x16x32_bf16 v[112:115], a[208:211], v[68:71], v[112:115]\n\t"
        "v_mfma_f32_16x16x32_bf16 v[116:119], a[212:215], v[68:71], v[116:119]\n\t"
        "v_mfma_f32_16x16x32_bf16 v[120:123], a[216:219], v[68:71], v[120:123]\n\t"
        "v_mfma_f32_16x16x32_bf16 v[124:127], a[220:223], v[68:71], v[124:127]\n\t"
        "buffer_load_dwordx4 v[64:67], v2, s[56:59], s22 offen offset:3072 sc1\n\t"
        "s_waitcnt vmcnt(10)\n\t"
        "v_mfma_f32_16x16x32_bf16 v[112:115], a[224:227], v[72:75], v[112:115]\n\t"
        "v_mfma_f32_16x16x32_bf16 v[116:119], a[228:231], v[72:75], v[116:119]\n\t"
        "v_mfma_f32_16x16x32_bf16 v[120:123], a[232:235], v[72:75], v[120:123]\n\t"
        "v_mfma_f32_16x16x32_bf16 v[124:127], a[236:239], v[72:75], v[124:127]\n\t"
        "s_add_u32 s22, s22, 4096\n\t"
        "buffer_load_dwordx4 v[68:71], v2, s[56:59], s22 offen offset:0 sc1\n\t"
        "s_waitcnt vmcnt(10)\n\t"
        "v_mfma_f32_16x16x32_bf16 v[112:115], a[240:243], v[76:79], v[112:115]\n\t"
        "v_mfma_f32_16x16x32_bf16 v[116:119], a[244:247], v[76:79], v[116:119]\n\t"
        "v_mfma_f32_16x16x32_bf16 v[120:123], a[248:251], v[76:79], v[120:123]\n\t"
        "v_mfma_f32_16x16x32_bf16 v[124:127], a[252:255], v[76:79], v[124:127]\n\t"
        "buffer_load_dwordx4 v[72:75], v2, s[56:59], s22 offen offset:1024 sc1\n\t"
        "s_waitcnt vmcnt(10)\n\t"
        "v_mfma_f32_16x16x32_bf16 v[112:115], v[24:27], v[80:83], v[112:115]\n\t"
        "v_mfma_f32_16x16x32_bf16 v[116:119], v[28:31], v[80:83], v[116:119]\n\t"
        "v_mfma_f32_16x16x32_bf16 v[120:123], v[32:35], v[80:83], v[120:123]\n\t"
        "v_mfma_f32_16x16x32_bf16 v[124:127], v[36:39], v[80:83], v[124:127]\n\t"
        "buffer_load_dwordx4 v[76:79], v40, s[56:59], s22 offen offset:2048 sc1\n\t"
        "s_waitcnt vmcnt(10)\n\t"
        "v_mfma_f32_16x16x32_bf16 v[96:99], a[96:99], v[84:87], 0\n\t"
        "v_mfma_f32_16x16x32_bf16 v[100:103], a[100:103], v[84:87], 0\n\t"
        "v_mfma_f32_16x16x32_bf16 v[104:107], a[104:107], v[84:87], 0\n\t"
        "v_mfma_f32_16x16x32_bf16 v[108:111], a[108:111], v[84:87], 0\n\t"
        "buffer_load_dwordx4 v[80:83], v2, s[56:59], s23 offen offset:0 sc1\n\t"
        "s_waitcnt vmcnt(10)\n\t"
        "v_mfma_f32_16x16x32_bf16 v[96:99], a[112:115], v[88:91], v[96:99]\n\t"
        "v_mfma_f32_16x16x32_bf16 v[100:103], a[116:119], v[88:91], v[100:103]\n\t"
        "v_mfma_f32_16x16x32_bf16 v[104:107], a[120:123], v[88:91], v[104:107]\n\t"
        "v_mfma_f32_16x16x32_bf16 v[108:111], a[124:127], v[88:91], v[108:111]\n\t"
        "buffer_load_dwordx4 v[84:87], v2, s[56:59], s23 offen offset:1024 sc1\n\t"
        "s_waitcnt vmcnt(10)\n\t"
        "v_mfma_f32_16x16x32_bf16 v[96:99], a[128:131], v[44:47], v[96:99]\n\t"
        "ds_write_b128 v11, v[112:115] offset:0\n\t"
        "v_mfma_f32_16x16x32_bf16 v[100:103], a[132:135], v[44:47], v[100:103]\n\t"
        "ds_write_b128 v11, v[116:119] offset:1024\n\t"
        "v_mfma_f32_16x16x32_bf16 v[104:107], a[136:139], v[44:47], v[104:107]\n\t"
        "ds_write_b128 v11, v[120:123] offset:2048\n\t"
        "v_mfma_f32_16x16x32_bf16 v[108:111], a[140:143], v[44:47], v[108:111]\n\t"
        "ds_write_b128 v11, v[124:127] offset:3072\n\t"
        "buffer_load_dwordx4 v[88:91], v2, s[56:59], s23 offen offset:2048 sc1\n\t"
        "s_waitcnt vmcnt(10)\n\t"
        "v_mfma_f32_16x16x32_bf16 v[96:99], a[144:147], v[48:51], v[96:99]\n\t"
        "v_mfma_f32_16x16x32_bf16 v[100:103], a[148:151], v[48:51], v[100:103]\n\t"
        "v_mfma_f32_16x16x32_bf16 v[104:107], a[152:155], v[48:51], v[104:107]\n\t"
        "v_mfma_f32_16x16x32_bf16 v[108:111], a[156:159], v[48:51], v[108:111]\n\t"
        "buffer_load_dwordx4 v[44:47], v2, s[56:59], s23 offen offset:3072 sc1\n\t"
        "s_waitcnt vmcnt(10)\n\t"
        "v_mfma_f32_16x16x32_bf16 v[96:99], a[160:163], v[52:55], v[96:99]\n\t"
        "v_mfma_f32_16x16x32_bf16 v[100:103], a[164:167], v[52:55], v[100:103]\n\t"
        "v_mfma_f32_16x16x32_bf16 v[104:107], a[168:171], v[52:55], v[104:107]\n\t"
        "v_mfma_f32_16x16x32_bf16 v[108:111], a[172:175], v[52:55], v[108:111]\n\t"
        "s_add_u32 s23, s23, 4096\n\t"
        "buffer_load_dwordx4 v[48:51], v2, s[56:59], s23 offen offset:0 sc1\n\t"
        "s_waitcnt vmcnt(10)\n\t"
        "v_mfma_f32_16x16x32_bf16 v[96:99], a[176:179], v[56:59], v[96:99]\n\t"
        "v_mfma_f32_16x16x32_bf16 v[100:103], a[180:183], v[56:59], v[100:103]\n\t"
        "v_mfma_f32_16x16x32_bf16 v[104:107], a[184:187], v[56:59], v[104:107]\n\t"
        "v_mfma_f32_16x16x32_bf16 v[108:111], a[188:191], v[56:59], v[108:111]\n\t"
        "buffer_load_dwordx4 v[52:55], v2, s[56:59], s23 offen offset:1024 sc1\n\t"
        "s_waitcnt vmcnt(10)\n\t"
        "v_mfma_f32_16x16x32_bf16 v[96:99], a[192:195], v[60:63], v[96:99]\n\t"
        "v_mfma_f32_16x16x32_bf16 v[100:103], a[196:199], v[60:63], v[100:103]\n\t"
        "v_mfma_f32_16x16x32_bf16 v[104:107], a[200:203], v[60:63], v[104:107]\n\t"
        "v_mfma_f32_16x16x32_bf16 v[108:111], a[204:207], v[60:63], v[108:111]\n\t"
        "buffer_load_dwordx4 v[56:59], v2, s[56:59], s23 offen offset:2048 sc1\n\t"
        "s_waitcnt vmcnt(10)\n\t"
        "v_mfma_f32_16x16x32_bf16 v[96:99], a[208:211], v[64:67], v[96:99]\n\t"
        "v_mfma_f32_16x16x32_bf16 v[100:103], a[212:215], v[64:67], v[100:103]\n\t"
        "v_mfma_f32_16x16x32_bf16 v[104:107], a[216:219], v[64:67], v[104:107]\n\t"
        "v_mfma_f32_16x16x32_bf16 v[108:111], a[220:223], v[64:67], v[108:111]\n\t"
        "buffer_load_dwordx4 v[60:63], v2, s[56:59], s23 offen offset:3072 sc1\n\t"
        "s_waitcnt vmcnt(10)\n\t"
        "v_mfma_f32_16x16x32_bf16 v[96:99], a[224:227], v[68:71], v[96:99]\n\t"
        "v_mfma_f32_16x16x32_bf16 v[100:103], a[228:231], v[68:71], v[100:103]\n\t"
        "v_mfma_f32_16x16x32_bf16 v[104:107], a[232:235], v[68:71], v[104:107]\n\t"
        "v_mfma_f32_16x16x32_bf16 v[108:111], a[236:239], v[68:71], v[108:111]\n\t"
        "s_add_u32 s23, s23, 4096\n\t"
        "buffer_load_dwordx4 v[64:67], v2, s[56:59], s23 offen offset:0 sc1\n\t"
        "s_waitcnt vmcnt(10)\n\t"
        "v_mfma_f32_16x16x32_bf16 v[96:99], a[240:243], v[72:75], v[96:99]\n\t"
        "v_mfma_f32_16x16x32_bf16 v[100:103], a[244:247], v[72:75], v[100:103]\n\t"
        "v_mfma_f32_16x16x32_bf16 v[104:107], a[248:251], v[72:75], v[104:107]\n\t"
        "v_mfma_f32_16x16x32_bf16 v[108:111], a[252:255], v[72:75], v[108:111]\n\t"
        "buffer_load_dwordx4 v[68:71], v2, s[56:59], s23 offen offset:1024 sc1\n\t"
        "s_waitcnt vmcnt(10)\n\t"
        "v_mfma_f32_16x16x32_bf16 v[96:99], v[24:27], v[76:79], v[96:99]\n\t"
        "v_mfma_f32_16x16x32_bf16 v[100:103], v[28:31], v[76:79], v[100:103]\n\t"
        "v_mfma_f32_16x16x32_bf16 v[104:107], v[32:35], v[76:79], v[104:107]\n\t"
        "v_mfma_f32_16x16x32_bf16 v[108:111], v[36:39], v[76:79], v[108:111]\n\t"
        "buffer_load_dwordx4 v[72:75], v40, s[56:59], s23 offen offset:2048 sc1\n\t"
        "s_waitcnt vmcnt(10)\n\t"
        "v_mfma_f32_16x16x32_bf16 v[112:115], a[96:99], v[80:83], 0\n\t"
        "v_mfma_f32_16x16x32_bf16 v[116:119], a[100:103], v[80:83], 0\n\t"
        "v_mfma_f32_16x16x32_bf16 v[120:123], a[104:107], v[80:83], 0\n\t"
        "v_mfma_f32_16x16x32_bf16 v[124:127], a[108:111], v[80:83], 0\n\t"
        "s_waitcnt vmcnt(9)\n\t"
        "v_mfma_f32_16x16x32_bf16 v[112:115], a[112:115], v[84:87], v[112:115]\n\t"
        "v_mfma_f32_16x16x32_bf16 v[116:119], a[116:119], v[84:87], v[116:119]\n\t"
        "v_mfma_f32_16x16x32_bf16 v[120:123], a[120:123], v[84:87], v[120:123]\n\t"
        "v_mfma_f32_16x16x32_bf16 v[124:127], a[124:127], v[84:87], v[124:127]\n\t"
        "s_waitcnt vmcnt(8)\n\t"
        "v_mfma_f32_16x16x32_bf16 v[112:115], a[128:131], v[88:91], v[112:115]\n\t"
        "ds_write_b128 v12, v[96:99] offset:0\n\t"
        "v_mfma_f32_16x16x32_bf16 v[116:119], a[132:135], v[88:91], v[116:119]\n\t"
        "ds_write_b128 v12, v[100:103] offset:1024\n\t"
        "v_mfma_f32_16x16x32_bf16 v[120:123], a[136:139], v[88:91], v[120:123]\n\t"
        "ds_write_b128 v12, v[104:107] offset:2048\n\t"
        "v_mfma_f32_16x16x32_bf16 v[124:127], a[140:143], v[88:91], v[124:127]\n\t"
        "ds_write_b128 v12, v[108:111] offset:3072\n\t"
        "s_waitcnt vmcnt(7)\n\t"
        "v_mfma_f32_16x16x32_bf16 v[112:115], a[144:147], v[44:47], v[112:115]\n\t"
        "v_mfma_f32_16x16x32_bf16 v[116:119], a[148:151], v[44:47], v[116:119]\n\t"
        "v_mfma_f32_16x16x32_bf16 v[120:123], a[152:155], v[44:47], v[120:123]\n\t"
        "v_mfma_f32_16x16x32_bf16 v[124:127], a[156:159], v[44:47], v[124:127]\n\t"
        "s_waitcnt vmcnt(6)\n\t"
        "v_mfma_f32_16x16x32_bf16 v[112:115], a[160:163], v[48:51], v[112:115]\n\t"
        "v_mfma_f32_16x16x32_bf16 v[116:119], a[164:167], v[48:51], v[116:119]\n\t"
        "v_mfma_f32_16x16x32_bf16 v[120:123], a[168:171], v[48:51], v[120:123]\n\t"
        "v_mfma_f32_16x16x32_bf16 v[124:127], a[172:175], v[48:51], v[124:127]\n\t"
        "s_waitcnt vmcnt(5)\n\t"
        "v_mfma_f32_16x16x32_bf16 v[112:115], a[176:179], v[52:55], v[112:115]\n\t"
        "v_mfma_f32_16x16x32_bf16 v[116:119], a[180:183], v[52:55], v[116:119]\n\t"
        "v_mfma_f32_16x16x32_bf16 v[120:123], a[184:187], v[52:55], v[120:123]\n\t"
        "v_mfma_f32_16x16x32_bf16 v[124:127], a[188:191], v[52:55], v[124:127]\n\t"
        "s_waitcnt vmcnt(4)\n\t"
        "v_mfma_f32_16x16x32_bf16 v[112:115], a[192:195], v[56:59], v[112:115]\n\t"
        "v_mfma_f32_16x16x32_bf16 v[116:119], a[196:199], v[56:59], v[116:119]\n\t"
        "v_mfma_f32_16x16x32_bf16 v[120:123], a[200:203], v[56:59], v[120:123]\n\t"
        "v_mfma_f32_16x16x32_bf16 v[124:127], a[204:207], v[56:59], v[124:127]\n\t"
        "s_waitcnt vmcnt(3)\n\t"
        "v_mfma_f32_16x16x32_bf16 v[112:115], a[208:211], v[60:63], v[112:115]\n\t"
        "v_mfma_f32_16x16x32_bf16 v[116:119], a[212:215], v[60:63], v[116:119]\n\t"
        "v_mfma_f32_16x16x32_bf16 v[120:123], a[216:219], v[60:63], v[120:123]\n\t"
        "v_mfma_f32_16x16x32_bf16 v[124:127], a[220:223], v[60:63], v[124:127]\n\t"
        "s_waitcnt vmcnt(2)\n\t"
        "v_mfma_f32_16x16x32_bf16 v[112:115], a[224:227], v[64:67], v[112:115]\n\t"
        "v_mfma_f32_16x16x32_bf16 v[116:119], a[228:231], v[64:67], v[116:119]\n\t"
        "v_mfma_f32_16x16x32_bf16 v[120:123], a[232:235], v[64:67], v[120:123]\n\t"
        "v_mfma_f32_16x16x32_bf16 v[124:127], a[236:239], v[64:67], v[124:127]\n\t"
        "s_waitcnt vmcnt(1)\n\t"
        "v_mfma_f32_16x16x32_bf16 v[112:115], a[240:243], v[68:71], v[112:115]\n\t"
        "v_mfma_f32_16x16x32_bf16 v[116:119], a[244:247], v[68:71], v[116:119]\n\t"
        "v_mfma_f32_16x16x32_bf16 v[120:123], a[248:251], v[68:71], v[120:123]\n\t"
        "v_mfma_f32_16x16x32_bf16 v[124:127], a[252:255], v[68:71], v[124:127]\n\t"
        "s_waitcnt vmcnt(0)\n\t"
        "v_mfma_f32_16x16x32_bf16 v[112:115], v[24:27], v[72:75], v[112:115]\n\t"
        "v_mfma_f32_16x16x32_bf16 v[116:119], v[28:31], v[72:75], v[116:119]\n\t"
        "v_mfma_f32_16x16x32_bf16 v[120:123], v[32:35], v[72:75], v[120:123]\n\t"
        "v_mfma_f32_16x16x32_bf16 v[124:127], v[36:39], v[72:75], v[124:127]\n\t"
        "s_waitcnt lgkmcnt(0)\n\t"
        "s_barrier\n\t"
        "ds_read_b128 v[96:99], v13 offset:0\n\t"
        "ds_read_b128 v[100:103], v13 offset:1024\n\t"
        "ds_read_b128 v[104:107], v13 offset:2048\n\t"
        "ds_read_b128 v[108:111], v13 offset:3072\n\t"
        "ds_read_b128 v[160:163], v13 offset:4096\n\t"
        "ds_read_b128 v[164:167], v13 offset:5120\n\t"
        "ds_read_b128 v[168:171], v13 offset:6144\n\t"
        "ds_read_b128 v[172:175], v13 offset:7168\n\t"
        "s_waitcnt lgkmcnt(7)\n\t"
        "v_pk_add_f32 v[112:113], v[112:113], v[96:97]\n\t"
        "v_pk_add_f32 v[114:115], v[114:115], v[98:99]\n\t"
        "s_waitcnt lgkmcnt(6)\n\t"
        "v_pk_add_f32 v[116:117], v[116:117], v[100:101]\n\t"
        "v_pk_add_f32 v[118:119], v[118:119], v[102:103]\n\t"
        "s_waitcnt lgkmcnt(5)\n\t"
        "v_pk_add_f32 v[120:121], v[120:121], v[104:105]\n\t"
        "v_pk_add_f32 v[122:123], v[122:123], v[106:107]\n\t"
        "s_waitcnt lgkmcnt(4)\n\t"
        "v_pk_add_f32 v[124:125], v[124:125], v[108:109]\n\t"
        "v_pk_add_f32 v[126:127], v[126:127], v[110:111]\n\t"
        "ds_read_b128 v[96:99], v13 offset:8192\n\t"
        "ds_read_b128 v[100:103], v13 offset:9216\n\t"
        "ds_read_b128 v[104:107], v13 offset:10240\n\t"
        "ds_read_b128 v[108:111], v13 offset:11264\n\t"
        "s_waitcnt lgkmcnt(7)\n\t"
        "v_pk_add_f32 v[112:113], v[112:113], v[160:161]\n\t"
        "v_pk_add_f32 v[114:115], v[114:115], v[162:163]\n\t"
        "s_waitcnt lgkmcnt(6)\n\t"
        "v_pk_add_f32 v[116:117], v[116:117], v[164:165]\n\t"
        "v_pk_add_f32 v[118:119], v[118:119], v[166:167]\n\t"
        "s_waitcnt lgkmcnt(5)\n\t"
        "v_pk_add_f32 v[120:121], v[120:121], v[168:169]\n\t"
        "v_pk_add_f32 v[122:123], v[122:123], v[170:171]\n\t"
        "s_waitcnt lgkmcnt(4)\n\t"
        "v_pk_add_f32 v[124:125], v[124:125], v[172:173]\n\t"
        "v_pk_add_f32 v[126:127], v[126:127], v[174:175]\n\t"
        "s_waitcnt lgkmcnt(3)\n\t"
        "v_pk_add_f32 v[112:113], v[112:113], v[96:97]\n\t"
        "v_pk_add_f32 v[114:115], v[114:115], v[98:99]\n\t"
        "s_waitcnt lgkmcnt(2)\n\t"
        "v_pk_add_f32 v[116:117], v[116:117], v[100:101]\n\t"
        "v_pk_add_f32 v[118:119], v[118:119], v[102:103]\n\t"
        "s_waitcnt lgkmcnt(1)\n\t"
        "v_pk_add_f32 v[120:121], v[120:121], v[104:105]\n\t"
        "v_pk_add_f32 v[122:123], v[122:123], v[106:107]\n\t"
        "s_waitcnt lgkmcnt(0)\n\t"
        "v_pk_add_f32 v[124:125], v[124:125], v[108:109]\n\t"
        "v_pk_add_f32 v[126:127], v[126:127], v[110:111]\n\t"
        "v_permlane16_swap_b32 v112, v116\n\t"
        "v_permlane16_swap_b32 v113, v117\n\t"
        "v_permlane16_swap_b32 v114, v118\n\t"
        "v_permlane16_swap_b32 v115, v119\n\t"
        "v_permlane16_swap_b32 v120, v124\n\t"
        "v_permlane16_swap_b32 v121, v125\n\t"
        "v_permlane16_swap_b32 v122, v126\n\t"
        "v_permlane16_swap_b32 v123, v127\n\t"
        "v_lshlrev_b32 v42, 16, v16\n\t"
        "v_and_b32 v43, s42, v16\n\t"
        "v_pk_fma_f32 v[42:43], v[144:145], v[112:113], v[42:43]\n\t"
        "v_cvt_pk_bf16_f32 v128, v42, v43\n\t"
        "v_lshlrev_b32 v42, 16, v17\n\t"
        "v_and_b32 v43, s42, v17\n\t"
        "v_pk_fma_f32 v[42:43], v[146:147], v[114:115], v[42:43]\n\t"
        "v_cvt_pk_bf16_f32 v129, v42, v43\n\t"
        "v_lshlrev_b32 v42, 16, v18\n\t"
        "v_and_b32 v43, s42, v18\n\t"
        "v_pk_fma_f32 v[42:43], v[148:149], v[116:117], v[42:43]\n\t"
        "v_cvt_pk_bf16_f32 v130, v42, v43\n\t"
        "v_lshlrev_b32 v42, 16, v19\n\t"
        "v_and_b32 v43, s42, v19\n\t"
        "v_pk_fma_f32 v[42:43], v[150:151], v[118:119], v[42:43]\n\t"
        "v_cvt_pk_bf16_f32 v131, v42, v43\n\t"
        "buffer_store_dwordx4 v[128:131], v5, s[76:79], s14 offen offset:0\n\t"
        "v_lshlrev_b32 v42, 16, v20\n\t"
        "v_and_b32 v43, s42, v20\n\t"
        "v_pk_fma_f32 v[42:43], v[152:153], v[120:121], v[42:43]\n\t"
        "v_cvt_pk_bf16_f32 v132, v42, v43\n\t"
        "v_lshlrev_b32 v42, 16, v21\n\t"
        "v_and_b32 v43, s42, v21\n\t"
        "v_pk_fma_f32 v[42:43], v[154:155], v[122:123], v[42:43]\n\t"
        "v_cvt_pk_bf16_f32 v133, v42, v43\n\t"
        "v_lshlrev_b32 v42, 16, v22\n\t"
        "v_and_b32 v43, s42, v22\n\t"
        "v_pk_fma_f32 v[42:43], v[156:157], v[124:125], v[42:43]\n\t"
        "v_cvt_pk_bf16_f32 v134, v42, v43\n\t"
        "v_lshlrev_b32 v42, 16, v23\n\t"
        "v_and_b32 v43, s42, v23\n\t"
        "v_pk_fma_f32 v[42:43], v[158:159], v[126:127], v[42:43]\n\t"
        "v_cvt_pk_bf16_f32 v135, v42, v43\n\t"
        "buffer_store_dwordx4 v[132:135], v5, s[76:79], s14 offen offset:64\n\t"
        "fused_code_end:\n\t"
        : "+v"(tid)
        : "s"(__builtin_amdgcn_kernarg_segment_ptr()), "s"(blockIdx.x)
        : "v1", "v2", "v3", "v4", "v5", "v6", "v7", "v8", "v9", "v10", "v11", "v12", "v13", "v14", "v15", "v16",
          "v17", "v18", "v19", "v20", "v21", "v22", "v23", "v24", "v25", "v26", "v27", "v28", "v29", "v30", "v31", "v32",
          "v33", "v34", "v35", "v36", "v37", "v38", "v39", "v40", "v41", "v42", "v43", "v44", "v45", "v46", "v47", "v48",
          "v49", "v50", "v51", "v52", "v53", "v54", "v55", "v56", "v57", "v58", "v59", "v60", "v61", "v62", "v63", "v64",
          "v65", "v66", "v67", "v68", "v69", "v70", "v71", "v72", "v73", "v74", "v75", "v76", "v77", "v78", "v79", "v80",
          "v81", "v82", "v83", "v84", "v85", "v86", "v87", "v88", "v89", "v90", "v91", "v92", "v93", "v94", "v95", "v96",
          "v97", "v98", "v99", "v100", "v101", "v102", "v103", "v104", "v105", "v106", "v107", "v108", "v109", "v110", "v111", "v112",
          "v113", "v114", "v115", "v116", "v117", "v118", "v119", "v120", "v121", "v122", "v123", "v124", "v125", "v126", "v127", "v128",
          "v129", "v130", "v131", "v132", "v133", "v134", "v135", "v136", "v137", "v138", "v139", "v140", "v141", "v142", "v143", "v144",
          "v145", "v146", "v147", "v148", "v149", "v150", "v151", "v152", "v153", "v154", "v155", "v156", "v157", "v158", "v159", "v160",
          "v161", "v162", "v163", "v164", "v165", "v166", "v167", "v168", "v169", "v170", "v171", "v172", "v173", "v174", "v175", "v176",
          "v177", "v178", "v179", "v180", "v181", "v182", "v183", "v184", "v185", "v186", "v187", "v188", "v189", "v190", "v191", "v192",
          "v193", "v194", "v195", "v196", "v197", "v198", "v199", "v200", "v201", "v202", "v203", "v204", "v205", "v206", "v207", "v208",
          "v209", "v210", "v211", "v212", "v213", "v214", "v215", "v216", "v217", "v218", "v219", "v220", "v221", "v222", "v223", "v224",
          "v225", "v226", "v227", "v228", "v229", "v230", "v231", "v232", "v233", "v234", "v235", "v236", "v237", "v238", "v239", "v240",
          "v241", "v242", "v243", "v244", "v245", "v246", "v247", "v248", "v249", "v250", "v251", "v252", "v253", "v254", "v255", "a0",
          "a1", "a2", "a3", "a4", "a5", "a6", "a7", "a8", "a9", "a10", "a11", "a12", "a13", "a14", "a15", "a16",
          "a17", "a18", "a19", "a20", "a21", "a22", "a23", "a24", "a25", "a26", "a27", "a28", "a29", "a30", "a31", "a32",
          "a33", "a34", "a35", "a36", "a37", "a38", "a39", "a40", "a41", "a42", "a43", "a44", "a45", "a46", "a47", "a48",
          "a49", "a50", "a51", "a52", "a53", "a54", "a55", "a56", "a57", "a58", "a59", "a60", "a61", "a62", "a63", "a64",
          "a65", "a66", "a67", "a68", "a69", "a70", "a71", "a72", "a73", "a74", "a75", "a76", "a77", "a78", "a79", "a80",
          "a81", "a82", "a83", "a84", "a85", "a86", "a87", "a88", "a89", "a90", "a91", "a92", "a93", "a94", "a95", "a96",
          "a97", "a98", "a99", "a100", "a101", "a102", "a103", "a104", "a105", "a106", "a107", "a108", "a109", "a110", "a111", "a112",
          "a113", "a114", "a115", "a116", "a117", "a118", "a119", "a120", "a121", "a122", "a123", "a124", "a125", "a126", "a127", "a128",
          "a129", "a130", "a131", "a132", "a133", "a134", "a135", "a136", "a137", "a138", "a139", "a140", "a141", "a142", "a143", "a144",
          "a145", "a146", "a147", "a148", "a149", "a150", "a151", "a152", "a153", "a154", "a155", "a156", "a157", "a158", "a159", "a160",
          "a161", "a162", "a163", "a164", "a165", "a166", "a167", "a168", "a169", "a170", "a171", "a172", "a173", "a174", "a175", "a176",
          "a177", "a178", "a179", "a180", "a181", "a182", "a183", "a184", "a185", "a186", "a187", "a188", "a189", "a190", "a191", "a192",
          "a193", "a194", "a195", "a196", "a197", "a198", "a199", "a200", "a201", "a202", "a203", "a204", "a205", "a206", "a207", "a208",
          "a209", "a210", "a211", "a212", "a213", "a214", "a215", "a216", "a217", "a218", "a219", "a220", "a221", "a222", "a223", "a224",
          "a225", "a226", "a227", "a228", "a229", "a230", "a231", "a232", "a233", "a234", "a235", "a236", "a237", "a238", "a239", "a240",
          "a241", "a242", "a243", "a244", "a245", "a246", "a247", "a248", "a249", "a250", "a251", "a252", "a253", "a254", "a255", "s4",
          "s5", "s6", "s7", "s8", "s9", "s10", "s11", "s12", "s13", "s14", "s15", "s16", "s17", "s18", "s19", "s20",
          "s21", "s22", "s23", "s24", "s25", "s26", "s27", "s28", "s29", "s30", "s31", "s33", "s34", "s35", "s36", "s37",
          "s38", "s39", "s40", "s41", "s42", "s43", "s44", "s45", "s46", "s47", "s48", "s49", "s50", "s51", "s52", "s53",
          "s54", "s55", "s56", "s57", "s58", "s59", "s60", "s61", "s62", "s63", "s64", "s65", "s66", "s67", "s68", "s69",
          "s70", "s71", "s72", "s73", "s74", "s75", "s76", "s77", "s78", "s79", "s80", "s81", "s82", "s83", "s84", "s85",
          "s86", "s87", "s88", "s89", "s90", "s91", "s92", "s93", "s94", "s95", "s96", "s97", "s98", "s99", "vcc", "scc",
          "memory");
}

extern "C" void kernel_launch(void* const* d_in, const int* in_sizes, int n_in, void* d_out, int out_size, void* d_ws, size_t ws_size,
                              hipStream_t stream) {
    fused_block<<<256, 256, 0, stream>>>((const u16*)d_in[0], (const u16*)d_in[1], (const u16*)d_in[2], (const u16*)d_in[4],
                                         (const u16*)d_in[5], (const u16*)d_in[6], (const u16*)d_in[7], (u16*)d_out,
                                         (unsigned char*)d_ws);
}
```
